```python
import torch

def _softmax(x, axis=-1):
    return torch.softmax(x, dim=axis)
def _gelu(x):
    return torch.nn.functional.gelu(x, approximate="tanh")
def _silu(x):
    return torch.nn.functional.silu(x)
def _sigmoid(x):
    return torch.sigmoid(x)
def _layernorm(x, axis=-1, eps=1e-5):
    m = x.mean(dim=axis, keepdim=True)
    v = x.var(dim=axis, keepdim=True, unbiased=False)
    return (x - m) / torch.sqrt(v + eps)
def _scatter_add(tgt, idx, src, axis=0):
    return tgt.clone().index_add_(axis, idx, src)
def _segment_sum(src, seg, n):
    out = torch.zeros((n,) + src.shape[1:], dtype=src.dtype)
    return out.index_add_(0, seg, src)
def _pad(x, pw):
    flat = tuple(v for pair in reversed(pw) for v in pair)
    return torch.nn.functional.pad(x, flat)

kernel_name = 'parallel_sum__ssm_conv_h3__moe_arch_mod__prompt'

def setup_inputs():

    _w = (lambda a: (lambda t: t.to(torch.bfloat16) if t.is_floating_point() else t)(torch.as_tensor(a)))
    def _A_setup():
        B, S, D = 1, 2048, 1024
        ks = 4
        return {
            "q": _w((lambda *s: torch.randn(*s, dtype=torch.bfloat16) * 0.05)(B, S, D)),
            "k": _w((lambda *s: torch.randn(*s, dtype=torch.bfloat16) * 0.8)(B, S, D)),
            "v": _w((lambda *s: torch.randn(*s, dtype=torch.bfloat16) * 0.8)(B, S, D)),
            "shift_k": _w((lambda *s: torch.randn(*s, dtype=torch.bfloat16) * 0.8)(D, ks) * 0.5),
            "ssm_k": _w((lambda *s: torch.randn(*s, dtype=torch.bfloat16) * 0.8)(D, S) * (1.0 / S ** 0.5)),
            "ssm_D": _w((lambda *s: torch.randn(*s, dtype=torch.bfloat16) * 0.8)(D)),
        }
    _A_in = _A_setup()

    def _B_setup():
        B, S, D, F, C = 1, 2048, 1024, 3584, 2
        return {
            "x": _w((lambda *s: torch.randn(*s, dtype=torch.bfloat16) * 0.05)(B, S, D)),
            "w_r": _w((lambda *s: torch.randn(*s, dtype=torch.bfloat16) * 0.05)(D)),
            "w_gate": _w((lambda *s: torch.randn(*s, dtype=torch.bfloat16) * 0.05)(D, F)),
            "w_up": _w((lambda *s: torch.randn(*s, dtype=torch.bfloat16) * 0.05)(D, F)),
            "w_down": _w((lambda *s: torch.randn(*s, dtype=torch.bfloat16) * 0.05)(F, D)),
        }
    _B_in = _B_setup()

    return {
        "A_q": _A_in["q"],
        "A_k": _A_in["k"],
        "A_v": _A_in["v"],
        "A_shift_k": _A_in["shift_k"],
        "A_ssm_k": _A_in["ssm_k"],
        "A_ssm_D": _A_in["ssm_D"],
        "B_x": _B_in["x"],
        "B_w_r": _B_in["w_r"],
        "B_w_gate": _B_in["w_gate"],
        "B_w_up": _B_in["w_up"],
        "B_w_down": _B_in["w_down"],
    }

def reference(A_q, A_k, A_v, A_shift_k, A_ssm_k, A_ssm_D, B_x, B_w_r, B_w_gate, B_w_up, B_w_down):
    def _A_ref(q, k, v, shift_k, ssm_k, ssm_D):
        B, S, D = q.shape
        ks = shift_k.shape[-1]
        k_pad = _pad(k, ((0, 0), (ks - 1, 0), (0, 0)))
        k_shift = torch.zeros_like(k)
        for t in range(ks):
            k_shift = k_shift + k_pad[:, t:t + S, :] * shift_k[:, ks - 1 - t][None, None, :]
        kv = k_shift * v
        L = 2 * S
        kv_pad = _pad(kv, ((0, 0), (0, S), (0, 0)))
        kf_pad = _pad(ssm_k, ((0, 0), (0, S)))
        kv_f = (lambda x, axes=(-2,-1): torch.fft.rfft2(x.float(), dim=axes))(kv_pad[:, None, :, :], axes=(1, 2))
        kf_f = (lambda x, axes=(-2,-1): torch.fft.rfft2(x.float(), dim=axes))(kf_pad[:, None, :], axes=(1, 2))
        kf_f = (lambda x, *d: x.permute(*d))(kf_f, 1, 2, 0)
        y_f = kv_f * kf_f[None, :, :, :]
        y = (lambda x, s, axes=(-2,-1): torch.fft.irfft2(x, s=s, dim=axes).to(torch.bfloat16))(y_f, (1, L), axes=(1, 2))[:, 0, :S, :]
        y = y + kv * ssm_D[None, None, :]
        return q * y

    def _B_ref(x, w_r, w_gate, w_up, w_down):
        B, S, D = x.shape
        C = 2
        r = x @ w_r
        idx = torch.argsort(-r, axis=-1)[:, :C]
        idx_e = (lambda x, n, axis: torch.repeat_interleave(x, n, dim=axis))(idx.reshape(B, C, 1), D, axis=2)
        x_sel = (lambda x, i, axis: torch.take_along_dim(x, i, dim=axis))(x, idx_e, axis=1)
        g = _silu(x_sel @ w_gate)
        u = x_sel @ w_up
        y = (g * u) @ w_down
        r_sel = _sigmoid((lambda x, i, axis: torch.take_along_dim(x, i, dim=axis))(r, idx, axis=1))
        y = y * r_sel.reshape(B, C, 1)
        b_off = (torch.arange(B) * S).reshape(B, 1)
        flat_idx = (idx + b_off).reshape(B * C)
        zero = (x * 0.0).reshape(B * S, D)
        upd = _scatter_add(zero, flat_idx, y.reshape(B * C, D), axis=0)
        return x + upd.reshape(B, S, D)

    x = A_q
    xn = _layernorm(x, axis=-1)
    return x + _A_ref(xn, A_k, A_v, A_shift_k, A_ssm_k, A_ssm_D) + _B_ref(xn, B_w_r, B_w_gate, B_w_up, B_w_down)
```

```cpp
#include <hip/hip_runtime.h>
#include <hip/hip_bf16.h>
#include <stdint.h>

typedef unsigned short u16;
typedef unsigned int u32;
typedef __attribute__((ext_vector_type(8))) __bf16 bf16x8;
typedef __attribute__((ext_vector_type(4))) float f32x4;
typedef __attribute__((ext_vector_type(4))) u32 u32x4;
typedef __attribute__((ext_vector_type(2))) u32 u32x2;

constexpr int S = 2048, D = 1024, F = 3584;

__device__ __forceinline__ float bf2f(u16 v) { return __uint_as_float(((u32)v) << 16); }
__device__ __forceinline__ float lo2f(u32 v) { return __uint_as_float(v << 16); }
__device__ __forceinline__ float hi2f(u32 v) { return __uint_as_float(v & 0xffff0000u); }
__device__ __forceinline__ u16 f2bf(float f) { __bf16 b = (__bf16)f; return __builtin_bit_cast(u16, b); }
__device__ __forceinline__ float rbf(float f) { return bf2f(f2bf(f)); }
__device__ __forceinline__ u32 pack2(float lo, float hi) { return (u32)f2bf(lo) | ((u32)f2bf(hi) << 16); }

__device__ __forceinline__ float wave_sum(float v) {
#pragma unroll
    for (int o = 32; o > 0; o >>= 1) v += __shfl_xor(v, o);
    return v;
}

__global__ void __launch_bounds__(256) k_prep(const u16* __restrict__ q, const u16* __restrict__ k, const u16* __restrict__ v,
                                              const u16* __restrict__ shk, const u16* __restrict__ wr,
                                              u16* __restrict__ kvT, float* __restrict__ stats, float* __restrict__ rsc) {
    __shared__ float red[4][8];
    const int t = threadIdx.x, b = blockIdx.x, lane = t & 63, w = t >> 6;
    const int r0 = b * 8, c0 = t * 4;
    float x[8][4];
#pragma unroll
    for (int r = 0; r < 8; ++r) {
        u32x2 a = *(const u32x2*)(q + (size_t)(r0 + r) * D + c0);
        x[r][0] = lo2f(a.x); x[r][1] = hi2f(a.x); x[r][2] = lo2f(a.y); x[r][3] = hi2f(a.y);
    }
    float kk[11][4], vv[8][4];
#pragma unroll
    for (int r = 0; r < 11; ++r) {
        int row = r0 + r - 3;
        u32x2 a = {0u, 0u};
        if (row >= 0) a = *(const u32x2*)(k + (size_t)row * D + c0);
        kk[r][0] = lo2f(a.x); kk[r][1] = hi2f(a.x); kk[r][2] = lo2f(a.y); kk[r][3] = hi2f(a.y);
    }
#pragma unroll
    for (int r = 0; r < 8; ++r) {
        u32x2 a = *(const u32x2*)(v + (size_t)(r0 + r) * D + c0);
        vv[r][0] = lo2f(a.x); vv[r][1] = hi2f(a.x); vv[r][2] = lo2f(a.y); vv[r][3] = hi2f(a.y);
    }
    float mean[8], var[8];
#pragma unroll
    for (int r = 0; r < 8; ++r) {
        float s = wave_sum(x[r][0] + x[r][1] + x[r][2] + x[r][3]);
        if (lane == 0) red[w][r] = s;
    }
    __syncthreads();
#pragma unroll
    for (int r = 0; r < 8; ++r) mean[r] = (red[0][r] + red[1][r] + red[2][r] + red[3][r]) * (1.0f / D);
    __syncthreads();
#pragma unroll
    for (int r = 0; r < 8; ++r) {
        float s = 0;
#pragma unroll
        for (int j = 0; j < 4; ++j) { float d = x[r][j] - mean[r]; s += d * d; }
        s = wave_sum(s);
        if (lane == 0) red[w][r] = s;
    }
    __syncthreads();
#pragma unroll
    for (int r = 0; r < 8; ++r) var[r] = (red[0][r] + red[1][r] + red[2][r] + red[3][r]) * (1.0f / D);
    __syncthreads();
    float wrf[4];
    { u32x2 a = *(const u32x2*)(wr + c0); wrf[0] = lo2f(a.x); wrf[1] = hi2f(a.x); wrf[2] = lo2f(a.y); wrf[3] = hi2f(a.y); }
#pragma unroll
    for (int r = 0; r < 8; ++r) {
        float m = rbf(mean[r]);
        float sd = rbf(sqrtf(rbf(rbf(var[r]) + 1e-5f)));
        float s = 0;
#pragma unroll
        for (int j = 0; j < 4; ++j) { float xn = rbf(rbf(x[r][j] - m) / sd); s += xn * wrf[j]; }
        s = wave_sum(s);
        if (lane == 0) red[w][r] = s;
        if (t == 0) { stats[(r0 + r) * 2] = m; stats[(r0 + r) * 2 + 1] = sd; }
    }
    __syncthreads();
    if (t < 8) rsc[r0 + t] = rbf(red[0][t] + red[1][t] + red[2][t] + red[3][t]);
#pragma unroll
    for (int j = 0; j < 4; ++j) {
        u32x2 a = *(const u32x2*)(shk + (size_t)(c0 + j) * 4);
        float s0 = lo2f(a.x), s1 = hi2f(a.x), s2 = lo2f(a.y), s3 = hi2f(a.y);
        float kv[8];
#pragma unroll
        for (int r = 0; r < 8; ++r) {
            float acc = rbf(kk[r][j] * s3);
            acc = rbf(acc + rbf(kk[r + 1][j] * s2));
            acc = rbf(acc + rbf(kk[r + 2][j] * s1));
            acc = rbf(acc + rbf(kk[r + 3][j] * s0));
            kv[r] = acc * vv[r][j];
        }
        u32x4 o = {pack2(kv[7], kv[6]), pack2(kv[5], kv[4]), pack2(kv[3], kv[2]), pack2(kv[1], kv[0])};
        *(u32x4*)(kvT + (size_t)(c0 + j) * S + r0) = o;
    }
}

constexpr int HCOPY_SLOTS = 257 * 8 + 6;
constexpr int U_SLOTS = 32 + 256;
constexpr int CH_SLOTS = ((HCOPY_SLOTS + U_SLOTS + 15) / 16) * 16;
__device__ __forceinline__ int hbase(int s) { return 257 * s + 2 * (s >> 1); }

__global__ void __launch_bounds__(256) k_conv(const u16* __restrict__ kvT, const u16* __restrict__ ssk, const u16* __restrict__ ssD,
                                              u16* __restrict__ yT) {
    extern __shared__ __attribute__((aligned(16))) u32x4 lds[];
    const int t = threadIdx.x, lane = t & 63, w = t >> 6;
    const int ch = blockIdx.x * 4 + w;
    u32x4* U = lds + w * CH_SLOTS;
    u32x4* H = U + U_SLOTS;
    const u32x4* hrow = (const u32x4*)(ssk + (size_t)ch * S);
    const u32x4* urow = (const u32x4*)(kvT + (size_t)ch * S);
    u32x4 zero = {0u, 0u, 0u, 0u};
    u32x4 uu[4], C[4], P[4];
#pragma unroll
    for (int m = 0; m < 4; ++m) {
        int c = lane + 64 * m;
        uu[m] = urow[c];
        C[m] = hrow[c];
        P[m] = (c > 0) ? hrow[c - 1] : zero;
    }
    if (lane < 32) U[lane] = zero;
    if (lane < 8) H[hbase(lane)] = zero;
#pragma unroll
    for (int m = 0; m < 4; ++m) {
        int c = lane + 64 * m;
        U[32 + c] = uu[m];
        u32 W[8] = {P[m].x, P[m].y, P[m].z, P[m].w, C[m].x, C[m].y, C[m].z, C[m].w};
        u32 O[7];
#pragma unroll
        for (int e = 0; e < 7; ++e) O[e] = __builtin_amdgcn_alignbit(W[e + 1], W[e], 16);
#pragma unroll
        for (int s = 0; s < 8; ++s) {
            u32x4 o;
            if ((s + 1) & 1) { int e = s >> 1; o = (u32x4){O[e], O[e + 1], O[e + 2], O[e + 3]}; }
            else { int e = (s + 1) >> 1; o = (u32x4){W[e], W[e + 1], W[e + 2], W[e + 3]}; }
            H[hbase(s) + c + 1] = o;
        }
    }
    __syncthreads();
    const int i = lane & 15, g = lane >> 4;
    const u32x4* Ap = H + hbase(i & 7) + (i >> 3) + g;
    const u32x4* Bp = U + 32 + 2 * i + 1 - g;
    f32x4 acc[8];
#pragma unroll
    for (int n = 0; n < 8; ++n) acc[n] = (f32x4){0.f, 0.f, 0.f, 0.f};
#pragma unroll 2
    for (int k0 = 0; k0 < 8; ++k0) {
        bf16x8 A[8], B[8];
#pragma unroll
        for (int m = 0; m < 8; ++m) {
            A[m] = __builtin_bit_cast(bf16x8, Ap[32 * m + 4 * k0]);
            B[m] = __builtin_bit_cast(bf16x8, Bp[32 * m - 4 * k0]);
        }
#pragma unroll
        for (int n = 0; n < 8; ++n)
#pragma unroll
            for (int k1 = 0; k1 <= n; ++k1)
                acc[n] = __builtin_amdgcn_mfma_f32_16x16x32_bf16(A[k1], B[n - k1], acc[n], 0, 0, 0);
    }
    const float dd = bf2f(ssD[ch]);
    const u16* Ue = (const u16*)(U + 32);
#pragma unroll
    for (int n = 0; n < 8; ++n) {
        int t0 = 256 * n + 16 * i + 4 * g;
        float o[4];
#pragma unroll
        for (int r = 0; r < 4; ++r) {
            int tt = t0 + r;
            float kv = bf2f(Ue[(tt & ~7) + 7 - (tt & 7)]);
            o[r] = rbf(acc[n][r]) + rbf(kv * dd);
        }
        u32x2 p = {pack2(o[0], o[1]), pack2(o[2], o[3])};
        *(u32x2*)(yT + (size_t)ch * S + t0) = p;
    }
}

__device__ __forceinline__ void top2_block(const float* __restrict__ rsc, int* sidx, float* sval) {
    __shared__ float bv[2][4];
    __shared__ int bi[2][4];
    const int t = threadIdx.x, lane = t & 63, w = t >> 6;
    float v1 = -INFINITY, v2 = -INFINITY; int i1 = 0x7fffffff, i2 = 0x7fffffff;
    auto better = [](float a, int ia, float b, int ib) { return a > b || (a == b && ia < ib); };
    for (int e = 0; e < S / 256; ++e) {
        int idx = t + e * 256; float v = rsc[idx];
        if (better(v, idx, v1, i1)) { v2 = v1; i2 = i1; v1 = v; i1 = idx; }
        else if (better(v, idx, v2, i2)) { v2 = v; i2 = idx; }
    }
#pragma unroll
    for (int o = 32; o > 0; o >>= 1) {
        float a1 = __shfl_xor(v1, o), a2 = __shfl_xor(v2, o); int j1 = __shfl_xor(i1, o), j2 = __shfl_xor(i2, o);
        if (better(a1, j1, v1, i1)) {
            if (better(a2, j2, v1, i1)) { v2 = a2; i2 = j2; } else { v2 = v1; i2 = i1; }
            v1 = a1; i1 = j1;
        } else if (better(a1, j1, v2, i2)) { v2 = a1; i2 = j1; }
    }
    if (lane == 0) { bv[0][w] = v1; bv[1][w] = v2; bi[0][w] = i1; bi[1][w] = i2; }
    __syncthreads();
    if (t == 0) {
        v1 = bv[0][0]; v2 = bv[1][0]; i1 = bi[0][0]; i2 = bi[1][0];
        for (int ww = 1; ww < 4; ++ww) {
            float a1 = bv[0][ww], a2 = bv[1][ww]; int j1 = bi[0][ww], j2 = bi[1][ww];
            if (better(a1, j1, v1, i1)) {
                if (better(a2, j2, v1, i1)) { v2 = a2; i2 = j2; } else { v2 = v1; i2 = i1; }
                v1 = a1; i1 = j1;
            } else if (better(a1, j1, v2, i2)) { v2 = a1; i2 = j1; }
        }
        if (i1 >= S) i1 = 0;
        if (i2 >= S) i2 = (i1 == 0) ? 1 : 0;
        sidx[0] = i1; sidx[1] = i2; sval[0] = v1; sval[1] = v2;
    }
    __syncthreads();
}

__global__ void __launch_bounds__(256) k_ffn(const u16* __restrict__ q, const float* __restrict__ stats, const float* __restrict__ rsc,
                                             const u16* __restrict__ wg, const u16* __restrict__ wu, const u16* __restrict__ wd,
                                             float* __restrict__ part, int* __restrict__ selidx, float* __restrict__ selval) {
    __shared__ int sidx[2]; __shared__ float sval[2];
    __shared__ float xs[2][D];
    __shared__ float red[4][2][2][16];
    __shared__ float hh[2][16];
    const int t = threadIdx.x, lane = t & 63, w = t >> 6;
    top2_block(rsc, sidx, sval);
    if (blockIdx.x == 0 && t < 2) { selidx[t] = sidx[t]; selval[t] = sval[t]; }
    for (int tok = 0; tok < 2; ++tok) {
        int row = sidx[tok]; float m = stats[row * 2], sd = stats[row * 2 + 1];
        for (int c = t; c < D; c += 256) xs[tok][c] = rbf(rbf(bf2f(q[(size_t)row * D + c]) - m) / sd);
    }
    __syncthreads();
    const int f0 = blockIdx.x * 16;
    const int half = t & 1, rr = t >> 1;
    float ag[2][8] = {}, au[2][8] = {};
#pragma unroll
    for (int p = 0; p < 8; ++p) {
        int row = rr + p * 128;
        u32x4 a = *(const u32x4*)(wg + (size_t)row * F + f0 + half * 8);
        u32x4 b = *(const u32x4*)(wu + (size_t)row * F + f0 + half * 8);
        float x0 = xs[0][row], x1 = xs[1][row];
#pragma unroll
        for (int e = 0; e < 4; ++e) {
            float l = lo2f(a[e]), h = hi2f(a[e]);
            ag[0][2 * e] += x0 * l; ag[0][2 * e + 1] += x0 * h; ag[1][2 * e] += x1 * l; ag[1][2 * e + 1] += x1 * h;
            l = lo2f(b[e]); h = hi2f(b[e]);
            au[0][2 * e] += x0 * l; au[0][2 * e + 1] += x0 * h; au[1][2 * e] += x1 * l; au[1][2 * e + 1] += x1 * h;
        }
    }
#pragma unroll
    for (int tok = 0; tok < 2; ++tok)
#pragma unroll
        for (int e = 0; e < 8; ++e) {
            float a = ag[tok][e], b = au[tok][e];
#pragma unroll
            for (int o = 32; o > 1; o >>= 1) { a += __shfl_xor(a, o); b += __shfl_xor(b, o); }
            if (lane < 2) { red[w][0][tok][lane * 8 + e] = a; red[w][1][tok][lane * 8 + e] = b; }
        }
    __syncthreads();
    if (t < 32) {
        int tok = t >> 4, c = t & 15;
        float g = rbf(red[0][0][tok][c] + red[1][0][tok][c] + red[2][0][tok][c] + red[3][0][tok][c]);
        float u = rbf(red[0][1][tok][c] + red[1][1][tok][c] + red[2][1][tok][c] + red[3][1][tok][c]);
        float sg = rbf(g / (1.0f + __expf(-g)));
        hh[tok][c] = rbf(sg * u);
    }
    __syncthreads();
    float y[2][4] = {};
#pragma unroll
    for (int f = 0; f < 16; ++f) {
        u32x2 a = *(const u32x2*)(wd + (size_t)(f0 + f) * D + t * 4);
        float wv[4] = {lo2f(a.x), hi2f(a.x), lo2f(a.y), hi2f(a.y)};
#pragma unroll
        for (int j = 0; j < 4; ++j) { y[0][j] += hh[0][f] * wv[j]; y[1][j] += hh[1][f] * wv[j]; }
    }
#pragma unroll
    for (int tok = 0; tok < 2; ++tok)
        *(f32x4*)(part + ((size_t)blockIdx.x * 2 + tok) * D + t * 4) = (f32x4){y[tok][0], y[tok][1], y[tok][2], y[tok][3]};
}

__global__ void __launch_bounds__(256) k_final(const u16* __restrict__ q, const float* __restrict__ stats, const u16* __restrict__ yT,
                                               const float* __restrict__ part, const int* __restrict__ selidx, const float* __restrict__ selval,
                                               u16* __restrict__ out) {
    __shared__ u16 tile[64][66];
    __shared__ float upd[2][64];
    __shared__ float pr[2][2][64];
    const int t = threadIdx.x;
    const int c0 = blockIdx.x * 64, r0 = blockIdx.y * 64;
    {
        int ch = t >> 2, seg = t & 3;
        const u32x4* p = (const u32x4*)(yT + (size_t)(c0 + ch) * S + r0 + seg * 16);
        u32x4 a = p[0], b = p[1];
#pragma unroll
        for (int e = 0; e < 4; ++e) {
            tile[seg * 16 + 2 * e][ch] = (u16)a[e]; tile[seg * 16 + 2 * e + 1][ch] = (u16)(a[e] >> 16);
            tile[seg * 16 + 8 + 2 * e][ch] = (u16)b[e]; tile[seg * 16 + 8 + 2 * e + 1][ch] = (u16)(b[e] >> 16);
        }
    }
    const int i0 = selidx[0], i1 = selidx[1];
    const bool has = (i0 >= r0 && i0 < r0 + 64) || (i1 >= r0 && i1 < r0 + 64);
    if (has) {
        int c = t & 63, tok = (t >> 6) & 1, hf = t >> 7;
        float s = 0;
        for (int p = hf * 112; p < hf * 112 + 112; ++p) s += part[((size_t)p * 2 + tok) * D + c0 + c];
        pr[hf][tok][c] = s;
        __syncthreads();
        if (t < 128) {
            float y = rbf(pr[0][tok][c] + pr[1][tok][c]);
            float rs = rbf(1.0f / (1.0f + __expf(-selval[tok])));
            upd[tok][c] = rbf(y * rs);
        }
    }
    __syncthreads();
    {
        int r = t >> 2, seg = t & 3;
        int row = r0 + r;
        float m = stats[row * 2], sd = stats[row * 2 + 1];
        const u32x4* p = (const u32x4*)(q + (size_t)row * D + c0 + seg * 16);
        u32x4 a[2] = {p[0], p[1]}, o[2];
#pragma unroll
        for (int h = 0; h < 2; ++h)
#pragma unroll
            for (int e = 0; e < 4; ++e) {
                float res[2];
#pragma unroll
                for (int z = 0; z < 2; ++z) {
                    int c = seg * 16 + h * 8 + 2 * e + z;
                    float x = z ? hi2f(a[h][e]) : lo2f(a[h][e]);
                    float xn = rbf(rbf(x - m) / sd);
                    float A = rbf(xn * bf2f(tile[r][c]));
                    float B = xn;
                    if (row == i0) B = rbf(xn + upd[0][c]);
                    if (row == i1) B = rbf(xn + upd[1][c]);
                    res[z] = rbf(x + A) + B;
                }
                o[h][e] = pack2(res[0], res[1]);
            }
        u32x4* po = (u32x4*)(out + (size_t)row * D + c0 + seg * 16);
        po[0] = o[0]; po[1] = o[1];
    }
}

extern "C" void kernel_launch(void* const* d_in, const int* in_sizes, int n_in,
                              void* d_out, int out_size, void* d_ws, size_t ws_size,
                              hipStream_t stream) {
    const u16* q = (const u16*)d_in[0];
    const u16* k = (const u16*)d_in[1];
    const u16* v = (const u16*)d_in[2];
    const u16* shk = (const u16*)d_in[3];
    const u16* ssk = (const u16*)d_in[4];
    const u16* ssD = (const u16*)d_in[5];
    const u16* wr = (const u16*)d_in[7];
    const u16* wg = (const u16*)d_in[8];
    const u16* wu = (const u16*)d_in[9];
    const u16* wd = (const u16*)d_in[10];
    char* ws = (char*)d_ws;
    u16* kvT = (u16*)ws;
    u16* yT = (u16*)(ws + (4u << 20));
    float* part = (float*)(ws + (8u << 20));
    float* stats = (float*)(ws + (10u << 20));
    float* rsc = (float*)(ws + (10u << 20) + 16384);
    int* selidx = (int*)(ws + (10u << 20) + 32768);
    float* selval = (float*)(ws + (10u << 20) + 32768 + 64);
    constexpr int LDS_CONV = 4 * CH_SLOTS * 16;
    hipFuncSetAttribute((const void*)k_conv, hipFuncAttributeMaxDynamicSharedMemorySize, LDS_CONV);
    k_prep<<<256, 256, 0, stream>>>(q, k, v, shk, wr, kvT, stats, rsc);
    k_ffn<<<224, 256, 0, stream>>>(q, stats, rsc, wg, wu, wd, part, selidx, selval);
    k_conv<<<256, 256, LDS_CONV, stream>>>(kvT, ssk, ssD, yT);
    k_final<<<dim3(16, 32), 256, 0, stream>>>(q, stats, yT, part, selidx, selval, (u16*)d_out);
}
```

```cpp
#include <hip/hip_runtime.h>
#include <stdint.h>

typedef unsigned short u16;
typedef unsigned int u32;
typedef unsigned long long u64;
typedef __attribute__((ext_vector_type(8))) __bf16 bf16x8;
typedef __attribute__((ext_vector_type(4))) short s16x4;
typedef __attribute__((ext_vector_type(4))) float f32x4;
typedef __attribute__((ext_vector_type(2))) float f32x2;
typedef __attribute__((ext_vector_type(4))) u32 u32x4;
typedef __attribute__((ext_vector_type(2))) u32 u32x2;
typedef __attribute__((ext_vector_type(2))) __bf16 bf16x2;
#define LAS __attribute__((address_space(3)))
typedef __amdgpu_buffer_rsrc_t rsrc_t;

constexpr int S = 2048, D = 1024, F = 3584;
constexpr int SC1 = 16;
constexpr u32 EMPTY = 0xFFFFFFFFu;
constexpr int SPIN_CAP = 1 << 18;

constexpr u32 WS_EPOCH = 0, WS_RG = 4096, WS_SG = 16384, WS_KV = 1u << 20, WS_Y = 5u << 20, WS_P = 9u << 20, WS_TS = 12u << 20, WS_BYTES = 13u << 20;
constexpr int U_BYTES = 288 * 16, CH_BYTES = (288 + 2064) * 16;
constexpr int L_XS = 4 * CH_BYTES, L_RED = L_XS + 4096, L_CTL = L_RED + 4096, L_STAT = L_CTL + 64, L_TOP = L_STAT + 64, L_XOWN = L_TOP + 32,
              L_HB = L_XOWN + 32, L_END = L_HB + 256;
enum { C_CLEAR = 0, C_E = 1, C_ROUTER = 2, C_XSEL = 3, C_F = 4, C_Y = 5, C_KV = 6, C_SEL0 = 8, C_SEL1 = 9 };
__device__ __forceinline__ int hbase(int s) { return 257 * s + 2 * (s >> 1); }

__device__ __forceinline__ float lo2f(u32 v) { return __uint_as_float(v << 16); }
__device__ __forceinline__ float hi2f(u32 v) { return __uint_as_float(v & 0xffff0000u); }
__device__ __forceinline__ u32 pack2(float lo, float hi) { bf16x2 b = {(__bf16)lo, (__bf16)hi}; return __builtin_bit_cast(u32, b); }
__device__ __forceinline__ float rbf(float f) { return lo2f(pack2(f, 0.f)); }
__device__ __forceinline__ u32 umin(u32 a, u32 b) { return a < b ? a : b; }
__device__ __forceinline__ u32 umax(u32 a, u32 b) { return a > b ? a : b; }
__device__ __forceinline__ u32 noempty(u32 a) { return umin(a, EMPTY - 1u); }
__device__ __forceinline__ u32 max4(u32x4 a) { return umax(umax(a.x, a.y), umax(a.z, a.w)); }
__device__ __forceinline__ rsrc_t mkrsrc(const void* p, u32 bytes) { return __builtin_amdgcn_make_buffer_rsrc((void*)p, 0, bytes, 0x00020000); }

template <int CTRL> __device__ __forceinline__ u32 dppu(u32 v) { return (u32)__builtin_amdgcn_update_dpp(0, (int)v, CTRL, 0xf, 0xf, true); }
template <int CTRL> __device__ __forceinline__ float dppf(float v) { return __uint_as_float(dppu<CTRL>(__float_as_uint(v))); }
__device__ __forceinline__ float xrow_sum(float v) {
    auto a = __builtin_amdgcn_permlane16_swap(__float_as_uint(v), __float_as_uint(v), false, false);
    v = __uint_as_float(a[0]) + __uint_as_float(a[1]);
    auto b = __builtin_amdgcn_permlane32_swap(__float_as_uint(v), __float_as_uint(v), false, false);
    return __uint_as_float(b[0]) + __uint_as_float(b[1]);
}
__device__ __forceinline__ float wave_sum(float v) {
    v += dppf<0xB1>(v); v += dppf<0x4E>(v); v += dppf<0x141>(v); v += dppf<0x140>(v);
    return xrow_sum(v);
}
__device__ __forceinline__ void top2_merge(u32& hi, u32& lo, u32 ohi, u32 olo) {
    u32 nlo = umax(umin(hi, ohi), umax(lo, olo));
    hi = umax(hi, ohi); lo = nlo;
}

__device__ __forceinline__ LAS u32* ctl(char* sm, int i) { return (LAS u32*)(sm + L_CTL) + i; }
__device__ __forceinline__ void lds_signal(char* sm, int i, int lane) {
    asm volatile("s_waitcnt lgkmcnt(0)" ::: "memory");
    if (lane == 0) __hip_atomic_fetch_add(ctl(sm, i), 1u, __ATOMIC_RELAXED, __HIP_MEMORY_SCOPE_WORKGROUP);
    asm volatile("" ::: "memory");
}
__device__ __forceinline__ void lds_wait(char* sm, int i, u32 want) {
    _Pragma("nounroll") for (int it = 0; it < SPIN_CAP; ++it) {
        if ((u32)__builtin_amdgcn_readfirstlane(__hip_atomic_load(ctl(sm, i), __ATOMIC_RELAXED, __HIP_MEMORY_SCOPE_WORKGROUP)) >= want) break;
        __builtin_amdgcn_s_sleep(1);
    }
    asm volatile("" ::: "memory");
}

#define PIN() do { asm volatile("" ::: "memory"); __builtin_amdgcn_sched_barrier(0); } while (0)
#define TS(k) PIN()
#define TS_DECL do {} while (0)
#define TS_OUT do {} while (0)

struct KV { u32 k[11], v[8]; u32x4 sh; };
__device__ __forceinline__ void kv_load(KV& a, rsrc_t rk, rsrc_t rv, rsrc_t rsh, int rb, int t8) {
#pragma unroll
    for (int r = 3; r < 11; ++r) a.k[r] = __builtin_amdgcn_raw_buffer_load_b32(rk, t8 * 4, (rb * 8 + r - 3) * 2048, 0);
#pragma unroll
    for (int r = 0; r < 8; ++r) a.v[r] = __builtin_amdgcn_raw_buffer_load_b32(rv, t8 * 4, (rb * 8 + r) * 2048, 0);
#pragma unroll
    for (int r = 0; r < 3; ++r) a.k[r] = __builtin_amdgcn_raw_buffer_load_b32(rk, t8 * 4, (rb > 0 ? rb * 8 + r - 3 : 0) * 2048, 0);
    a.sh = __builtin_amdgcn_raw_buffer_load_b128(rsh, t8 * 16, 0, 0);
}
__device__ __forceinline__ void kv_compute(const KV& a, int rb, u32x4& o0, u32x4& o1) {
    const u32 hm = rb > 0 ? 0xffffffffu : 0u;
    f32x2 kk[11];
#pragma unroll
    for (int r = 0; r < 11; ++r) { u32 x = r < 3 ? (a.k[r] & hm) : a.k[r]; kk[r] = (f32x2){lo2f(x), hi2f(x)}; }
    const f32x2 s0 = {lo2f(a.sh.x), lo2f(a.sh.z)}, s1 = {hi2f(a.sh.x), hi2f(a.sh.z)}, s2 = {lo2f(a.sh.y), lo2f(a.sh.w)}, s3 = {hi2f(a.sh.y), hi2f(a.sh.w)};
    f32x2 kv[8];
#pragma unroll
    for (int r = 0; r < 8; ++r) {
        f32x2 acc = kk[r] * s3;
        acc = __builtin_elementwise_fma(kk[r + 1], s2, acc);
        acc = __builtin_elementwise_fma(kk[r + 2], s1, acc);
        acc = __builtin_elementwise_fma(kk[r + 3], s0, acc);
        kv[r] = acc * (f32x2){lo2f(a.v[r]), hi2f(a.v[r])};
    }
#pragma unroll
    for (int e = 0; e < 4; ++e) {
        u32 x = noempty(pack2(kv[7 - 2 * e].x, kv[6 - 2 * e].x)), y = noempty(pack2(kv[7 - 2 * e].y, kv[6 - 2 * e].y));
        auto sw = __builtin_amdgcn_permlane32_swap(x, y, false, false);
        o0[e] = sw[0]; o1[e] = sw[1];
    }
}
__device__ __forceinline__ u32 kv_off(int wave8, int lane, int rb) {
    const int ch = wave8 * 128 + 2 * (lane & 31) + (lane >> 5);
    return WS_KV + (ch >> 2) * 16384 + rb * 64 + (ch & 3) * 16;
}

__device__ __forceinline__ void unpack16(float (&x)[16], u32x4 a, u32x4 b) {
#pragma unroll
    for (int e = 0; e < 4; ++e) { x[2 * e] = lo2f(a[e]); x[2 * e + 1] = hi2f(a[e]); x[8 + 2 * e] = lo2f(b[e]); x[9 + 2 * e] = hi2f(b[e]); }
}

__global__ void __launch_bounds__(768, 3)
fused_kernel(const u16* q, const u16* k, const u16* v, const u16* shk,
             const u16* ssk, const u16* ssD, const u16* wr, const u16* wg,
             const u16* wu, const u16* wd, u16* out, char* ws) {
    extern __shared__ __attribute__((aligned(16))) char sm[];
    const int tid = threadIdx.x, lane = tid & 63;
    const int wave = __builtin_amdgcn_readfirstlane(tid >> 6), role = wave >> 2, sub = wave & 3;
    const int b = blockIdx.x, rb = (b & 7) * 32 + (b >> 3);
    const rsrc_t rws = mkrsrc(ws, WS_BYTES);
    const u32x4 empty4 = {EMPTY, EMPTY, EMPTY, EMPTY};
#define CLRMASK 7
    const rsrc_t rclr = mkrsrc(ws, ((CLRMASK >> role) & 1) ? WS_BYTES : 0u);
    TS_DECL;
    TS(0);
    if (tid < 16) *ctl(sm, tid) = 0;
    asm volatile("s_waitcnt lgkmcnt(0)" ::: "memory");

    if (role == 2) {
        const rsrc_t rq = mkrsrc(q, S * D * 2);
#pragma unroll
        for (int m = 0; m < 4; ++m)
            __builtin_amdgcn_raw_buffer_store_b128(empty4, rclr, WS_KV + ((sub * 4 + m) * 16 + (lane >> 2)) * 16384 + rb * 64 + (lane & 3) * 16, 0, SC1);
        const u32 tag = __builtin_amdgcn_raw_buffer_load_b32(rws, WS_EPOCH, 0, SC1) * 1664525u + 1013904223u;
        u32x4 qa[2][2], wa[2];
#pragma unroll
        for (int r = 0; r < 2; ++r)
#pragma unroll
            for (int h = 0; h < 2; ++h) qa[r][h] = __builtin_amdgcn_raw_buffer_load_b128(rq, lane * 16 + h * 1024, (rb * 8 + sub * 2 + r) * 2048, 0);
#pragma unroll
        for (int h = 0; h < 2; ++h) wa[h] = *(const u32x4*)(wr + h * 512 + lane * 8);
        PIN();
        __builtin_amdgcn_s_barrier();
        PIN();
        asm volatile("s_waitcnt vmcnt(0)" ::: "memory");
        TS(1);
        lds_signal(sm, C_CLEAR, lane);
        float w16[16]; unpack16(w16, wa[0], wa[1]);
        u32 st[2], key[2];
#pragma unroll
        for (int r = 0; r < 2; ++r) {
            float x[16]; unpack16(x, qa[r][0], qa[r][1]);
            float s = 0;
#pragma unroll
            for (int e = 0; e < 16; ++e) s += x[e];
            const float mean = wave_sum(s) * (1.0f / D);
            s = 0;
#pragma unroll
            for (int e = 0; e < 16; ++e) { float d = x[e] - mean; s = fmaf(d, d, s); }
            const float var = wave_sum(s) * (1.0f / D);
            const float m = rbf(mean), sd = rbf(__builtin_amdgcn_sqrtf(rbf(rbf(var) + 1e-5f)));
            const float rs = __builtin_amdgcn_rcpf(sd);
            s = 0;
#pragma unroll
            for (int e = 0; e < 16; e += 2) {
                u32 d2 = pack2(x[e] - m, x[e + 1] - m);
                u32 n2 = pack2(lo2f(d2) * rs, hi2f(d2) * rs);
                s = fmaf(lo2f(n2), w16[e], s); s = fmaf(hi2f(n2), w16[e + 1], s);
            }
            const float rsc = wave_sum(s);
            u32 u = pack2(rsc, 0.f) & 0xffffu;
            u = (u & 0x8000u) ? (~u & 0xffffu) : (u | 0x8000u);
            key[r] = (rsc == rsc) ? u : 0u;
            st[r] = pack2(m, sd);
            if (lane == 0) *(LAS f32x2*)(sm + L_STAT + (sub * 2 + r) * 8) = (f32x2){m, rs};
        }
        if (lane < 3) {
            const u32 val = lane == 0 ? (key[0] | key[1] << 16) : (lane == 1 ? st[0] : st[1]);
            const u32 off = lane == 0 ? WS_RG + (rb * 4 + sub) * 8 : WS_SG + (rb * 8 + sub * 2 + lane - 1) * 8;
            __builtin_amdgcn_raw_buffer_store_b64((u32x2){val, tag}, rws, off, 0, SC1);
        }
        TS(2);
        const int j = sub * 64 + lane;
        u32x2 xq[8];
#pragma unroll
        for (int r = 0; r < 8; ++r) xq[r] = __builtin_amdgcn_raw_buffer_load_b64(rq, j * 8, (rb * 8 + r) * 2048, 0);
        u32 hi = 0, lo = 0;
        _Pragma("nounroll") for (int it = 0; it < SPIN_CAP; ++it) {
            asm volatile("" ::: "memory");
            const u32x4 g0 = __builtin_amdgcn_raw_buffer_load_b128(rws, WS_RG + sub * 2048 + lane * 16, 0, SC1);
            const u32x4 g1 = __builtin_amdgcn_raw_buffer_load_b128(rws, WS_RG + sub * 2048 + 1024 + lane * 16, 0, SC1);
            if (__all(g0.y == tag && g0.w == tag && g1.y == tag && g1.w == tag)) {
                const u32 base = 2047u - (sub * 512 + lane * 4);
                const u32 gv[4] = {g0.x, g0.z, g1.x, g1.z};
#pragma unroll
                for (int e = 0; e < 4; ++e) {
                    const u32 ib = base - (e >> 1) * 256 - (e & 1) * 2;
                    const u32 k0 = (gv[e] << 16) | ib, k1 = (gv[e] & 0xffff0000u) | (ib - 1);
                    top2_merge(hi, lo, umax(k0, k1), umin(k0, k1));
                }
                break;
            }
            __builtin_amdgcn_s_sleep(2);
        }
        top2_merge(hi, lo, dppu<0xB1>(hi), dppu<0xB1>(lo));
        top2_merge(hi, lo, dppu<0x4E>(hi), dppu<0x4E>(lo));
        top2_merge(hi, lo, dppu<0x141>(hi), dppu<0x141>(lo));
        top2_merge(hi, lo, dppu<0x140>(hi), dppu<0x140>(lo));
        { auto a = __builtin_amdgcn_permlane16_swap(hi, hi, false, false); auto c = __builtin_amdgcn_permlane16_swap(lo, lo, false, false);
          hi = a[0]; lo = c[0]; top2_merge(hi, lo, a[1], c[1]); }
        { auto a = __builtin_amdgcn_permlane32_swap(hi, hi, false, false); auto c = __builtin_amdgcn_permlane32_swap(lo, lo, false, false);
          hi = a[0]; lo = c[0]; top2_merge(hi, lo, a[1], c[1]); }
        if (lane == 0) *(LAS u32x2*)(sm + L_TOP + sub * 8) = (u32x2){hi, lo};
        lds_signal(sm, C_E, lane);
        lds_wait(sm, C_E, 4);
        {
            const u32x4 t0 = *(LAS u32x4*)(sm + L_TOP), t1 = *(LAS u32x4*)(sm + L_TOP + 16);
            hi = t0.x; lo = t0.y; top2_merge(hi, lo, t0.z, t0.w); top2_merge(hi, lo, t1.x, t1.y); top2_merge(hi, lo, t1.z, t1.w);
        }
        hi = __builtin_amdgcn_readfirstlane(hi); lo = __builtin_amdgcn_readfirstlane(lo);
        const int sel0 = 2047 - (hi & 2047), sel1 = 2047 - (lo & 2047);
        TS(3);
        if (sub == 3) {
            if (lane == 0) { *ctl(sm, C_SEL0) = hi; *ctl(sm, C_SEL1) = lo; }
            lds_signal(sm, C_ROUTER, lane);
            if (b == 0 && lane == 0) __builtin_amdgcn_raw_buffer_store_b32(tag, rws, WS_EPOCH, 0, SC1);
        }
        if (sub < 2) {
            const int row = sub == 0 ? sel0 : sel1;
            u32x4 a[2];
#pragma unroll
            for (int h = 0; h < 2; ++h) a[h] = __builtin_amdgcn_raw_buffer_load_b128(rq, lane * 16 + h * 1024, row * 2048, 0);
            u32x2 sg;
            _Pragma("nounroll") for (int it = 0; it < SPIN_CAP; ++it) {
                asm volatile("" ::: "memory");
                sg = __builtin_amdgcn_raw_buffer_load_b64(rws, WS_SG + row * 8, 0, SC1);
                if (__builtin_amdgcn_readfirstlane(sg.y) == tag) break;
            }
            const float m = lo2f(sg.x), rs = __builtin_amdgcn_rcpf(hi2f(sg.x));
#pragma unroll
            for (int h = 0; h < 2; ++h) {
                u32x4 o;
#pragma unroll
                for (int e = 0; e < 4; ++e) { u32 d2 = pack2(lo2f(a[h][e]) - m, hi2f(a[h][e]) - m); o[e] = pack2(lo2f(d2) * rs, hi2f(d2) * rs); }
                *(LAS u32x4*)(sm + L_XS + sub * 2048 + h * 1024 + lane * 16) = o;
                if (h * 64 + lane == (rb >> 1)) *(LAS u32x2*)(sm + L_XOWN + sub * 8) = (rb & 1) ? (u32x2){a[h].z, a[h].w} : (u32x2){a[h].x, a[h].y};
            }
            lds_signal(sm, C_XSEL, lane);
        }
        TS(4);
        f32x2 xn[8][2], sx[8][2];
#pragma unroll
        for (int r = 0; r < 8; ++r) {
            const f32x2 sv = *(LAS f32x2*)(sm + L_STAT + r * 8);
#pragma unroll
            for (int h = 0; h < 2; ++h) {
                const float x0 = lo2f(xq[r][h]), x1 = hi2f(xq[r][h]);
                u32 d2 = pack2(x0 - sv.x, x1 - sv.x);
                u32 n2 = pack2(lo2f(d2) * sv.y, hi2f(d2) * sv.y);
                xn[r][h] = (f32x2){lo2f(n2), hi2f(n2)};
                sx[r][h] = xn[r][h] + (f32x2){x0, x1};
            }
        }
#pragma unroll
        for (int r = 0; r < 8; ++r) asm volatile("" : "+v"(xn[r][0]), "+v"(xn[r][1]), "+v"(sx[r][0]), "+v"(sx[r][1]));
        lds_wait(sm, C_Y, 4);
        TS(5);
        u32x4 y[4];
        _Pragma("nounroll") for (int it = 0; it < SPIN_CAP; ++it) {
            asm volatile("" ::: "memory");
#pragma unroll
            for (int i = 0; i < 4; ++i) y[i] = __builtin_amdgcn_raw_buffer_load_b128(rws, WS_Y + rb * 16384 + j * 64 + i * 16, 0, SC1);
            if (__all(umax(umax(max4(y[0]), max4(y[1])), umax(max4(y[2]), max4(y[3]))) != EMPTY)) break;
        }
        TS(6);
        const rsrc_t ro = mkrsrc(out, S * D * 2);
#pragma unroll
        for (int r = 0; r < 8; ++r) {
            u32x2 o;
#pragma unroll
            for (int h = 0; h < 2; ++h) {
                const u32 yy = y[r & 3][(r >> 2) * 2 + h];
                const f32x2 res = __builtin_elementwise_fma(xn[r][h], (f32x2){lo2f(yy), hi2f(yy)}, sx[r][h]);
                o[h] = pack2(res.x, res.y);
            }
            const int row = rb * 8 + r;
            if (row != sel0 && row != sel1) __builtin_amdgcn_raw_buffer_store_b64(o, ro, j * 8, row * 2048, 0);
        }
        TS(7);
    } else if (role == 0) {
        const int ch = rb * 4 + sub;
        const int par = lane >> 5, mm = (lane >> 2) & 7;
        const u32 yoff = WS_Y + (sub * 64 + 2 * mm + par) * 16384 + rb * 64 + (lane & 3) * 16;
#pragma unroll
        for (int m = 0; m < 4; ++m) __builtin_amdgcn_raw_buffer_store_b128(empty4, rclr, yoff + m * 16 * 16384, 0, SC1);
        PIN();
        KV a;
        kv_load(a, mkrsrc(k, S * D * 2), mkrsrc(v, S * D * 2), mkrsrc(shk, D * 8), rb, tid);
        PIN();
        const rsrc_t rh = mkrsrc(ssk + (size_t)ch * S, S * 2);
        u32x4 C[4], P[4];
#pragma unroll
        for (int m = 0; m < 4; ++m) {
            C[m] = __builtin_amdgcn_raw_buffer_load_b128(rh, lane * 16 + m * 1024, 0, 0);
            P[m] = __builtin_amdgcn_raw_buffer_load_b128(rh, lane * 16 + m * 1024 - 16, 0, 0);
        }
        const u32 dbits = ssD[ch];
        PIN();
        __builtin_amdgcn_s_barrier();
        PIN();
        u32x4 o0, o1;
        kv_compute(a, rb, o0, o1);
        asm volatile("" : "+v"(o0), "+v"(o1));
        TS(1);
        lds_signal(sm, C_CLEAR, lane);
        lds_wait(sm, C_CLEAR, 12);
        { const u32 off = kv_off(wave, lane, rb);
          __builtin_amdgcn_raw_buffer_store_b128(o0, rws, off, 0, SC1);
          __builtin_amdgcn_raw_buffer_store_b128(o1, rws, off + 16 * 16384, 0, SC1); }
        TS(2);
        LAS u32x4* U = (LAS u32x4*)(sm + sub * CH_BYTES);
        LAS u32x4* H = U + 288;
        const u32x4 zero = {0u, 0u, 0u, 0u};
        if (lane < 32) U[lane] = zero;
        if (lane < 8) H[hbase(lane)] = zero;
#pragma unroll
        for (int m = 0; m < 4; ++m) {
            const u32 W[8] = {P[m].x, P[m].y, P[m].z, P[m].w, C[m].x, C[m].y, C[m].z, C[m].w};
            u32 O[7];
#pragma unroll
            for (int e = 0; e < 7; ++e) O[e] = __builtin_amdgcn_alignbit(W[e + 1], W[e], 16);
#pragma unroll
            for (int s = 0; s < 8; ++s) {
                const int e = (s + 1) >> 1;
                H[hbase(s) + lane + 64 * m + 1] = (s & 1) ? (u32x4){W[e], W[e + 1], W[e + 2], W[e + 3]} : (u32x4){O[e], O[e + 1], O[e + 2], O[e + 3]};
            }
        }
        const int i = lane & 15, g = lane >> 4;
        u32x4 AD;
        { const int jj = 15 - i - 8 * g;
#pragma unroll
          for (int e = 0; e < 4; ++e) AD[e] = jj == 2 * e ? dbits : (jj == 2 * e + 1 ? dbits << 16 : 0u); }
        TS(3);
        lds_wait(sm, C_ROUTER, 1);
        TS(4);
        u32x4 uu[4];
        _Pragma("nounroll") for (int it = 0; it < SPIN_CAP; ++it) {
            asm volatile("" ::: "memory");
#pragma unroll
            for (int m = 0; m < 4; ++m) uu[m] = __builtin_amdgcn_raw_buffer_load_b128(rws, WS_KV + rb * 16384 + (m * 64 + lane) * 64 + sub * 16, 0, SC1);
            if (__all(umax(umax(max4(uu[0]), max4(uu[1])), umax(max4(uu[2]), max4(uu[3]))) != EMPTY)) break;
        }
        TS(5);
#pragma unroll
        for (int m = 0; m < 4; ++m) U[32 + m * 64 + lane] = uu[m];
        lds_signal(sm, C_KV, lane);
        __builtin_amdgcn_s_setprio(2);
        const LAS u32x4* Ap = H + hbase(i & 7) + (i >> 3) + g;
        const LAS u32x4* Bp = U + 32 + 2 * i + 1 - g;
        f32x4 acc[8];
#pragma unroll
        for (int n = 0; n < 8; ++n)
            acc[n] = __builtin_amdgcn_mfma_f32_16x16x32_bf16(__builtin_bit_cast(bf16x8, AD), __builtin_bit_cast(bf16x8, Bp[32 * n]), (f32x4){0.f, 0.f, 0.f, 0.f}, 0, 0, 0);
#pragma unroll 1
        for (int k0 = 0; k0 < 8; ++k0) {
            bf16x8 A[8], B[8];
#pragma unroll
            for (int m = 0; m < 8; ++m) {
                A[m] = __builtin_bit_cast(bf16x8, Ap[32 * m + 4 * k0]);
                B[m] = __builtin_bit_cast(bf16x8, Bp[32 * m - 4 * k0]);
            }
#pragma unroll
            for (int n = 0; n < 8; ++n)
#pragma unroll
                for (int k1 = 0; k1 <= n; ++k1)
                    acc[n] = __builtin_amdgcn_mfma_f32_16x16x32_bf16(A[k1], B[n - k1], acc[n], 0, 0, 0);
        }
        TS(6);
        LAS char* Y = (LAS char*)sm + U_BYTES;
#pragma unroll
        for (int n = 0; n < 8; ++n)
            *(LAS u32x2*)(Y + sub * (CH_BYTES + 64) + n * 512 + lane * 8) = (u32x2){pack2(acc[n][0], acc[n][1]), pack2(acc[n][2], acc[n][3])};
        lds_signal(sm, C_Y, lane);
        lds_wait(sm, C_Y, 4);
        {
            const int qq = (lane >> 2) & 3, p = lane & 3, grp = (lane >> 4) & 1;
            LAS char* base = Y + qq * (CH_BYTES + 64) + sub * 1024 + par * 256 + (grp * 4 + p) * 8;
#pragma unroll
            for (int m = 0; m < 4; ++m) {
                LAS char* pa = base + (m >> 1) * 512 + (m & 1) * 64;
                const u32x2 r0 = __builtin_bit_cast(u32x2, __builtin_amdgcn_ds_read_tr16_b64_v4i16((LAS s16x4*)pa));
                const u32x2 r1 = __builtin_bit_cast(u32x2, __builtin_amdgcn_ds_read_tr16_b64_v4i16((LAS s16x4*)(pa + 128)));
                __builtin_amdgcn_raw_buffer_store_b128((u32x4){noempty(r0.x), noempty(r0.y), noempty(r1.x), noempty(r1.y)}, rws, yoff + m * 16 * 16384, 0, SC1);
            }
        }
        TS(7);
    } else {
        const bool has = (b >> 3) < 28;
        const int fs = has ? (b & 7) * 28 + (b >> 3) : 0;
        const int n = lane & 15, g = lane >> 4;
        const rsrc_t rp = mkrsrc(ws, has ? WS_BYTES : 0u);
        const u32 poff = WS_P + (sub * 32 + n) * (224 * 64) + fs * 64 + g * 16;
#pragma unroll
        for (int be = 0; be < 2; ++be) __builtin_amdgcn_raw_buffer_store_b128(empty4, (CLRMASK & 2) ? rp : rclr, poff + be * 16 * (224 * 64), 0, SC1);
        PIN();
        KV a;
        kv_load(a, mkrsrc(k, S * D * 2), mkrsrc(v, S * D * 2), mkrsrc(shk, D * 8), rb, tid);
        PIN();
        u32x4 G[2][8], Wd[2][4];
        {
            const bool hasw = has;
            const rsrc_t rd = mkrsrc(wd, hasw ? D * F * 2 : 0);
            const u32 rstride = hasw ? F * 2 : 0;
            const char* gp = (const char*)((n & 2) ? wu : wg) + (sub * 256 + ((n >> 2) * 4 + g) * 8) * rstride + fs * 32 + (n & 1) * 16;
#pragma unroll
            for (int be = 0; be < 2; ++be)
#pragma unroll
                for (int jj = 0; jj < 8; ++jj) G[be][jj] = *(const u32x4*)(gp + (be * 128 + jj) * rstride);
#pragma unroll
            for (int be = 0; be < 2; ++be)
#pragma unroll
                for (int jj = 0; jj < 4; ++jj)
                    Wd[be][jj] = __builtin_amdgcn_raw_buffer_load_b128(rd, (fs * 16 + 4 * g + jj) * 2048 + (sub * 2 + be) * 256 + n * 16, 0, 0);
        }
        PIN();
        __builtin_amdgcn_s_barrier();
        PIN();
        u32x4 o0, o1;
        kv_compute(a, rb, o0, o1);
        asm volatile("" : "+v"(o0), "+v"(o1));
        TS(1);
        lds_signal(sm, C_CLEAR, lane);
        lds_wait(sm, C_CLEAR, 12);
        { const u32 off = kv_off(wave, lane, rb);
          __builtin_amdgcn_raw_buffer_store_b128(o0, rws, off, 0, SC1);
          __builtin_amdgcn_raw_buffer_store_b128(o1, rws, off + 16 * 16384, 0, SC1); }
        TS(2);
        u32x4 GB[2][8]; u32x2 DB[2][8];
#pragma unroll
        for (int be = 0; be < 2; ++be)
#pragma unroll
            for (int e = 0; e < 4; ++e) {
#pragma unroll
                for (int pr = 0; pr < 4; ++pr) {
                    GB[be][2 * e][pr] = __builtin_amdgcn_perm(G[be][2 * pr + 1][e], G[be][2 * pr][e], 0x05040100u);
                    GB[be][2 * e + 1][pr] = __builtin_amdgcn_perm(G[be][2 * pr + 1][e], G[be][2 * pr][e], 0x07060302u);
                }
            }
#pragma unroll
        for (int be = 0; be < 2; ++be)
#pragma unroll
            for (int c = 0; c < 8; ++c) asm volatile("" : "+v"(GB[be][c]));
        TS(3);
        lds_wait(sm, C_XSEL, 2);
        TS(4);
        f32x4 ga[4];
#pragma unroll
        for (int c = 0; c < 4; ++c) ga[c] = (f32x4){0.f, 0.f, 0.f, 0.f};
#pragma unroll
        for (int be = 0; be < 2; ++be) {
            const u32x4 xa = *(LAS u32x4*)(sm + L_XS + (n & 1) * 2048 + (sub * 256 + be * 128 + ((n >> 2) * 4 + g) * 8) * 2);
            const u32x4 z4 = {0u, 0u, 0u, 0u};
            const u32x4 xe = (n & 2) ? z4 : xa, xo = (n & 2) ? xa : z4;
#pragma unroll
            for (int c = 0; c < 8; ++c)
                ga[c >> 1] = __builtin_amdgcn_mfma_f32_16x16x32_bf16(__builtin_bit_cast(bf16x8, (c & 1) ? xo : xe), __builtin_bit_cast(bf16x8, GB[be][c]), ga[c >> 1], 0, 0, 0);
        }
        if (g == (n >> 2)) {
#pragma unroll
            for (int c = 0; c < 4; ++c) *(LAS f32x4*)(sm + L_RED + (sub * 4 + g) * 256 + (n & 3) * 64 + c * 16) = ga[c];
        }
        lds_signal(sm, C_F, lane);
#pragma unroll
        for (int be = 0; be < 2; ++be)
#pragma unroll
            for (int e = 0; e < 4; ++e)
#pragma unroll
                for (int pr = 0; pr < 2; ++pr) {
                    DB[be][2 * e][pr] = __builtin_amdgcn_perm(Wd[be][2 * pr + 1][e], Wd[be][2 * pr][e], 0x05040100u);
                    DB[be][2 * e + 1][pr] = __builtin_amdgcn_perm(Wd[be][2 * pr + 1][e], Wd[be][2 * pr][e], 0x07060302u);
                }
#pragma unroll
        for (int be = 0; be < 2; ++be)
#pragma unroll
            for (int c = 0; c < 8; ++c) asm volatile("" : "+v"(DB[be][c]));
        lds_wait(sm, C_F, 4);
        {
            float s = 0;
#pragma unroll
            for (int p = 0; p < 16; ++p) s += *(LAS float*)(sm + L_RED + p * 256 + lane * 4);
            s = rbf(s);
            auto sw = __builtin_amdgcn_permlane32_swap(__float_as_uint(s), __float_as_uint(s), false, false);
            const float gg = __uint_as_float(sw[0]), up = __uint_as_float(sw[1]);
            const float hv = rbf(gg * __builtin_amdgcn_rcpf(1.0f + __expf(-gg))) * up;
            if (lane < 32) *(LAS u16*)(sm + L_HB + sub * 64 + (lane & 1) * 32 + (lane >> 1) * 2) = (u16)pack2(hv, 0.f);
        }
        const u32x2 ha = *(LAS u32x2*)(sm + L_HB + sub * 64 + (n & 1) * 32 + g * 8);
#pragma unroll
        for (int be = 0; be < 2; ++be) {
            f32x4 da = {0.f, 0.f, 0.f, 0.f};
#pragma unroll
            for (int c = 0; c < 8; ++c) {
                const u32x2 ac = (n >> 1) == c ? ha : (u32x2){0u, 0u};
                da = __builtin_amdgcn_mfma_f32_16x16x16bf16_1k(__builtin_bit_cast(s16x4, ac), __builtin_bit_cast(s16x4, DB[be][c]), da, 0, 0, 0);
            }
            __builtin_amdgcn_raw_buffer_store_b128((u32x4){noempty(__float_as_uint(da[0])), noempty(__float_as_uint(da[1])), noempty(__float_as_uint(da[2])), noempty(__float_as_uint(da[3]))},
                                                   rp, poff + be * 16 * (224 * 64), 0, SC1);
        }
        TS(5);
        if (sub == 0) {
            lds_wait(sm, C_KV, 1);
            u32x4 pp[7];
            _Pragma("nounroll") for (int it = 0; it < SPIN_CAP; ++it) {
                asm volatile("" ::: "memory");
                u32 mx = 0;
#pragma unroll
                for (int m = 0; m < 7; ++m) {
                    pp[m] = __builtin_amdgcn_raw_buffer_load_b128(rws, WS_P + (rb >> 1) * (224 * 64) + (m * 32 + (lane >> 1)) * 64 + (rb & 1) * 32 + (lane & 1) * 16, 0, SC1);
                    mx = umax(mx, max4(pp[m]));
                }
                if (__all(mx != EMPTY)) break;
                __builtin_amdgcn_s_sleep(2);
            }
            TS(6);
            float sv[4];
#pragma unroll
            for (int e = 0; e < 4; ++e) {
                float s = 0;
#pragma unroll
                for (int m = 0; m < 7; ++m) s += __uint_as_float(pp[m][e]);
                s += dppf<0x4E>(s); s += dppf<0x124>(s); s += dppf<0x128>(s);
                sv[e] = xrow_sum(s);
            }
            const int e = (lane >> 1) & 3, tok = e & 1, c = 2 * (lane & 1) + (e >> 1);
            const float yv = rbf(e == 0 ? sv[0] : (e == 1 ? sv[1] : (e == 2 ? sv[2] : sv[3])));
            const u32 kq = *ctl(sm, C_SEL0 + tok);
            const int row = 2047 - (kq & 2047);
            const u32 kb = kq >> 16;
            const float rv = lo2f((kb & 0x8000u) ? (kb & 0x7fffu) : (~kb & 0xffffu));
            const float upd = rbf(yv * rbf(__builtin_amdgcn_rcpf(1.0f + __expf(-rv))));
            const float x = lo2f(*(LAS u16*)(sm + L_XOWN + tok * 8 + c * 2));
            const float xn = lo2f(*(LAS u16*)(sm + L_XS + tok * 2048 + (rb * 4 + c) * 2));
            lds_wait(sm, C_Y, 4);
            const float y2 = lo2f(*(LAS u16*)(sm + U_BYTES + c * (CH_BYTES + 64) + (row >> 8) * 512 + (((row >> 2) & 3) * 16 + ((row >> 4) & 15)) * 8 + (row & 3) * 2));
            const float res = rbf(x + rbf(xn * y2)) + rbf(xn + upd);
            if (lane < 8) out[(size_t)row * D + rb * 4 + c] = (u16)pack2(res, 0.f);
            TS(7);
        }
    }
    TS_OUT;
}

extern "C" void kernel_launch(void* const* d_in, const int* in_sizes, int n_in, void* d_out, int out_size, void* d_ws, size_t ws_size,
                              hipStream_t stream) {
    (void)hipFuncSetAttribute((const void*)fused_kernel, hipFuncAttributeMaxDynamicSharedMemorySize, L_END);
    fused_kernel<<<256, 768, L_END, stream>>>((const u16*)d_in[0], (const u16*)d_in[1], (const u16*)d_in[2], (const u16*)d_in[3], (const u16*)d_in[4],
                                              (const u16*)d_in[5], (const u16*)d_in[7], (const u16*)d_in[8], (const u16*)d_in[9], (const u16*)d_in[10],
                                              (u16*)d_out, (char*)d_ws);
}
```

```cpp
#define POL_A 2
#define POL_W 0
#include <hip/hip_runtime.h>
#include <stdint.h>

typedef unsigned short u16;
typedef unsigned int u32;
typedef unsigned long long u64;
typedef __attribute__((ext_vector_type(8))) __bf16 bf16x8;
typedef __attribute__((ext_vector_type(4))) short s16x4;
typedef __attribute__((ext_vector_type(4))) float f32x4;
typedef __attribute__((ext_vector_type(2))) float f32x2;
typedef __attribute__((ext_vector_type(4))) u32 u32x4;
typedef __attribute__((ext_vector_type(2))) u32 u32x2;
typedef __attribute__((ext_vector_type(2))) __bf16 bf16x2;
#define LAS __attribute__((address_space(3)))
typedef __amdgpu_buffer_rsrc_t rsrc_t;

constexpr int S = 2048, D = 1024, F = 3584;
constexpr int SC1 = 16;
constexpr u32 EMPTY = 0xFFFFFFFFu;
constexpr int SPIN_CAP = 1 << 18;

constexpr u32 WS_EPOCH = 0, WS_RG = 4096, WS_SG = 16384, WS_KV = 1u << 20, WS_Y = 5u << 20, WS_P = 9u << 20, WS_TS = 12u << 20, WS_BYTES = 13u << 20;
constexpr int U_BYTES = 288 * 16, CH_BYTES = (288 + 2064) * 16;
constexpr int L_XS = 4 * CH_BYTES, L_RED = L_XS + 4096, L_CTL = L_RED + 4096, L_STAT = L_CTL + 64, L_TOP = L_STAT + 64, L_XOWN = L_TOP + 32,
              L_HB = L_XOWN + 32, L_END = L_HB + 256;
enum { C_CLEAR = 0, C_E = 1, C_ROUTER = 2, C_XSEL = 3, C_F = 4, C_Y = 5, C_KV = 6, C_SEL0 = 8, C_SEL1 = 9 };
__device__ __forceinline__ int hbase(int s) { return 257 * s + 2 * (s >> 1); }

__device__ __forceinline__ float lo2f(u32 v) { return __uint_as_float(v << 16); }
__device__ __forceinline__ float hi2f(u32 v) { return __uint_as_float(v & 0xffff0000u); }
__device__ __forceinline__ u32 pack2(float lo, float hi) { bf16x2 b = {(__bf16)lo, (__bf16)hi}; return __builtin_bit_cast(u32, b); }
__device__ __forceinline__ float rbf(float f) { return lo2f(pack2(f, 0.f)); }
__device__ __forceinline__ u32 umin(u32 a, u32 b) { return a < b ? a : b; }
__device__ __forceinline__ u32 umax(u32 a, u32 b) { return a > b ? a : b; }
__device__ __forceinline__ u32 noempty(u32 a) { return umin(a, EMPTY - 1u); }
__device__ __forceinline__ u32 max4(u32x4 a) { return umax(umax(a.x, a.y), umax(a.z, a.w)); }
__device__ __forceinline__ rsrc_t mkrsrc(const void* p, u32 bytes) { return __builtin_amdgcn_make_buffer_rsrc((void*)p, 0, bytes, 0x00020000); }

template <int CTRL> __device__ __forceinline__ u32 dppu(u32 v) { return (u32)__builtin_amdgcn_update_dpp(0, (int)v, CTRL, 0xf, 0xf, true); }
template <int CTRL> __device__ __forceinline__ float dppf(float v) { return __uint_as_float(dppu<CTRL>(__float_as_uint(v))); }
__device__ __forceinline__ float xrow_sum(float v) {
    auto a = __builtin_amdgcn_permlane16_swap(__float_as_uint(v), __float_as_uint(v), false, false);
    v = __uint_as_float(a[0]) + __uint_as_float(a[1]);
    auto b = __builtin_amdgcn_permlane32_swap(__float_as_uint(v), __float_as_uint(v), false, false);
    return __uint_as_float(b[0]) + __uint_as_float(b[1]);
}
__device__ __forceinline__ float wave_sum(float v) {
    v += dppf<0xB1>(v); v += dppf<0x4E>(v); v += dppf<0x141>(v); v += dppf<0x140>(v);
    return xrow_sum(v);
}
__device__ __forceinline__ void top2_merge(u32& hi, u32& lo, u32 ohi, u32 olo) {
    u32 nlo = umax(umin(hi, ohi), umax(lo, olo));
    hi = umax(hi, ohi); lo = nlo;
}

__device__ __forceinline__ LAS u32* ctl(char* sm, int i) { return (LAS u32*)(sm + L_CTL) + i; }
__device__ __forceinline__ void lds_signal(char* sm, int i, int lane) {
    asm volatile("s_waitcnt lgkmcnt(0)" ::: "memory");
    if (lane == 0) __hip_atomic_fetch_add(ctl(sm, i), 1u, __ATOMIC_RELAXED, __HIP_MEMORY_SCOPE_WORKGROUP);
    asm volatile("" ::: "memory");
}
__device__ __forceinline__ void lds_wait(char* sm, int i, u32 want) {
    _Pragma("nounroll") for (int it = 0; it < SPIN_CAP; ++it) {
        if ((u32)__builtin_amdgcn_readfirstlane(__hip_atomic_load(ctl(sm, i), __ATOMIC_RELAXED, __HIP_MEMORY_SCOPE_WORKGROUP)) >= want) break;
        __builtin_amdgcn_s_sleep(1);
    }
    asm volatile("" ::: "memory");
}

#define PIN() do { asm volatile("" ::: "memory"); __builtin_amdgcn_sched_barrier(0); } while (0)
#define TS(k) PIN()
#define TS_DECL do {} while (0)
#define TS_OUT do {} while (0)

struct KV { u32 k[11], v[8]; u32x4 sh; };
__device__ __forceinline__ void kv_load(KV& a, rsrc_t rk, rsrc_t rv, rsrc_t rsh, int rb, int t8) {
#pragma unroll
    for (int r = 3; r < 11; ++r) a.k[r] = __builtin_amdgcn_raw_buffer_load_b32(rk, t8 * 4, (rb * 8 + r - 3) * 2048, POL_A);
#pragma unroll
    for (int r = 0; r < 8; ++r) a.v[r] = __builtin_amdgcn_raw_buffer_load_b32(rv, t8 * 4, (rb * 8 + r) * 2048, POL_A);
#pragma unroll
    for (int r = 0; r < 3; ++r) a.k[r] = __builtin_amdgcn_raw_buffer_load_b32(rk, t8 * 4, (rb > 0 ? rb * 8 + r - 3 : 0) * 2048, POL_A);
    a.sh = __builtin_amdgcn_raw_buffer_load_b128(rsh, t8 * 16, 0, 0);
}
__device__ __forceinline__ void kv_compute(const KV& a, int rb, u32x4& o0, u32x4& o1) {
    const u32 hm = rb > 0 ? 0xffffffffu : 0u;
    f32x2 kk[11];
#pragma unroll
    for (int r = 0; r < 11; ++r) { u32 x = r < 3 ? (a.k[r] & hm) : a.k[r]; kk[r] = (f32x2){lo2f(x), hi2f(x)}; }
    const f32x2 s0 = {lo2f(a.sh.x), lo2f(a.sh.z)}, s1 = {hi2f(a.sh.x), hi2f(a.sh.z)}, s2 = {lo2f(a.sh.y), lo2f(a.sh.w)}, s3 = {hi2f(a.sh.y), hi2f(a.sh.w)};
    f32x2 kv[8];
#pragma unroll
    for (int r = 0; r < 8; ++r) {
        f32x2 acc = kk[r] * s3;
        acc = __builtin_elementwise_fma(kk[r + 1], s2, acc);
        acc = __builtin_elementwise_fma(kk[r + 2], s1, acc);
        acc = __builtin_elementwise_fma(kk[r + 3], s0, acc);
        kv[r] = acc * (f32x2){lo2f(a.v[r]), hi2f(a.v[r])};
    }
#pragma unroll
    for (int e = 0; e < 4; ++e) {
        u32 x = noempty(pack2(kv[7 - 2 * e].x, kv[6 - 2 * e].x)), y = noempty(pack2(kv[7 - 2 * e].y, kv[6 - 2 * e].y));
        auto sw = __builtin_amdgcn_permlane32_swap(x, y, false, false);
        o0[e] = sw[0]; o1[e] = sw[1];
    }
}
__device__ __forceinline__ u32 kv_off(int wave8, int lane, int rb) {
    const int ch = wave8 * 128 + 2 * (lane & 31) + (lane >> 5);
    return WS_KV + (ch >> 2) * 16384 + rb * 64 + (ch & 3) * 16;
}

__device__ __forceinline__ void unpack16(float (&x)[16], u32x4 a, u32x4 b) {
#pragma unroll
    for (int e = 0; e < 4; ++e) { x[2 * e] = lo2f(a[e]); x[2 * e + 1] = hi2f(a[e]); x[8 + 2 * e] = lo2f(b[e]); x[9 + 2 * e] = hi2f(b[e]); }
}

__global__ void __launch_bounds__(768, 3)
fused_kernel(const u16* q, const u16* k, const u16* v, const u16* shk,
             const u16* ssk, const u16* ssD, const u16* wr, const u16* wg,
             const u16* wu, const u16* wd, u16* out, char* ws) {
    extern __shared__ __attribute__((aligned(16))) char sm[];
    const int tid = threadIdx.x, lane = tid & 63;
    const int wave = __builtin_amdgcn_readfirstlane(tid >> 6), role = wave >> 2, sub = wave & 3;
    const int b = blockIdx.x, rb = (b & 7) * 32 + (b >> 3);
    const rsrc_t rws = mkrsrc(ws, WS_BYTES);
    const u32x4 empty4 = {EMPTY, EMPTY, EMPTY, EMPTY};
#define CLRMASK 7
    const rsrc_t rclr = mkrsrc(ws, ((CLRMASK >> role) & 1) ? WS_BYTES : 0u);
    TS_DECL;
    TS(0);
    if (tid < 16) *ctl(sm, tid) = 0;
    asm volatile("s_waitcnt lgkmcnt(0)" ::: "memory");

    if (role == 2) {
        const rsrc_t rq = mkrsrc(q, S * D * 2);
#pragma unroll
        for (int m = 0; m < 4; ++m)
            __builtin_amdgcn_raw_buffer_store_b128(empty4, rclr, WS_KV + ((sub * 4 + m) * 16 + (lane >> 2)) * 16384 + rb * 64 + (lane & 3) * 16, 0, SC1);
        const u32 tag = __builtin_amdgcn_raw_buffer_load_b32(rws, WS_EPOCH, 0, SC1) * 1664525u + 1013904223u;
        u32x4 qa[2][2], wa[2];
#pragma unroll
        for (int r = 0; r < 2; ++r)
#pragma unroll
            for (int h = 0; h < 2; ++h) qa[r][h] = __builtin_amdgcn_raw_buffer_load_b128(rq, lane * 16 + h * 1024, (rb * 8 + sub * 2 + r) * 2048, POL_A);
#pragma unroll
        for (int h = 0; h < 2; ++h) wa[h] = *(const u32x4*)(wr + h * 512 + lane * 8);
        PIN();
        __builtin_amdgcn_s_barrier();
        PIN();
        asm volatile("s_waitcnt vmcnt(0)" ::: "memory");
        TS(1);
        lds_signal(sm, C_CLEAR, lane);
        float w16[16]; unpack16(w16, wa[0], wa[1]);
        u32 st[2], key[2];
#pragma unroll
        for (int r = 0; r < 2; ++r) {
            float x[16]; unpack16(x, qa[r][0], qa[r][1]);
            float s = 0;
#pragma unroll
            for (int e = 0; e < 16; ++e) s += x[e];
            const float mean = wave_sum(s) * (1.0f / D);
            s = 0;
#pragma unroll
            for (int e = 0; e < 16; ++e) { float d = x[e] - mean; s = fmaf(d, d, s); }
            const float var = wave_sum(s) * (1.0f / D);
            const float m = rbf(mean), sd = rbf(__builtin_amdgcn_sqrtf(rbf(rbf(var) + 1e-5f)));
            const float rs = __builtin_amdgcn_rcpf(sd);
            s = 0;
#pragma unroll
            for (int e = 0; e < 16; e += 2) {
                u32 d2 = pack2(x[e] - m, x[e + 1] - m);
                u32 n2 = pack2(lo2f(d2) * rs, hi2f(d2) * rs);
                s = fmaf(lo2f(n2), w16[e], s); s = fmaf(hi2f(n2), w16[e + 1], s);
            }
            const float rsc = wave_sum(s);
            u32 u = pack2(rsc, 0.f) & 0xffffu;
            u = (u & 0x8000u) ? (~u & 0xffffu) : (u | 0x8000u);
            key[r] = (rsc == rsc) ? u : 0u;
            st[r] = pack2(m, sd);
            if (lane == 0) *(LAS f32x2*)(sm + L_STAT + (sub * 2 + r) * 8) = (f32x2){m, rs};
        }
        if (lane < 3) {
            const u32 val = lane == 0 ? (key[0] | key[1] << 16) : (lane == 1 ? st[0] : st[1]);
            const u32 off = lane == 0 ? WS_RG + (rb * 4 + sub) * 8 : WS_SG + (rb * 8 + sub * 2 + lane - 1) * 8;
            __builtin_amdgcn_raw_buffer_store_b64((u32x2){val, tag}, rws, off, 0, SC1);
        }
        TS(2);
        const int j = sub * 64 + lane;
        u32x2 xq[8];
#pragma unroll
        for (int r = 0; r < 8; ++r) xq[r] = __builtin_amdgcn_raw_buffer_load_b64(rq, j * 8, (rb * 8 + r) * 2048, POL_A);
        u32 hi = 0, lo = 0;
        _Pragma("nounroll") for (int it = 0; it < SPIN_CAP; ++it) {
            asm volatile("" ::: "memory");
            const u32x4 g0 = __builtin_amdgcn_raw_buffer_load_b128(rws, WS_RG + sub * 2048 + lane * 16, 0, SC1);
            const u32x4 g1 = __builtin_amdgcn_raw_buffer_load_b128(rws, WS_RG + sub * 2048 + 1024 + lane * 16, 0, SC1);
            if (__all(g0.y == tag && g0.w == tag && g1.y == tag && g1.w == tag)) {
                const u32 base = 2047u - (sub * 512 + lane * 4);
                const u32 gv[4] = {g0.x, g0.z, g1.x, g1.z};
#pragma unroll
                for (int e = 0; e < 4; ++e) {
                    const u32 ib = base - (e >> 1) * 256 - (e & 1) * 2;
                    const u32 k0 = (gv[e] << 16) | ib, k1 = (gv[e] & 0xffff0000u) | (ib - 1);
                    top2_merge(hi, lo, umax(k0, k1), umin(k0, k1));
                }
                break;
            }
            __builtin_amdgcn_s_sleep(2);
        }
        top2_merge(hi, lo, dppu<0xB1>(hi), dppu<0xB1>(lo));
        top2_merge(hi, lo, dppu<0x4E>(hi), dppu<0x4E>(lo));
        top2_merge(hi, lo, dppu<0x141>(hi), dppu<0x141>(lo));
        top2_merge(hi, lo, dppu<0x140>(hi), dppu<0x140>(lo));
        { auto a = __builtin_amdgcn_permlane16_swap(hi, hi, false, false); auto c = __builtin_amdgcn_permlane16_swap(lo, lo, false, false);
          hi = a[0]; lo = c[0]; top2_merge(hi, lo, a[1], c[1]); }
        { auto a = __builtin_amdgcn_permlane32_swap(hi, hi, false, false); auto c = __builtin_amdgcn_permlane32_swap(lo, lo, false, false);
          hi = a[0]; lo = c[0]; top2_merge(hi, lo, a[1], c[1]); }
        if (lane == 0) *(LAS u32x2*)(sm + L_TOP + sub * 8) = (u32x2){hi, lo};
        lds_signal(sm, C_E, lane);
        lds_wait(sm, C_E, 4);
        {
            const u32x4 t0 = *(LAS u32x4*)(sm + L_TOP), t1 = *(LAS u32x4*)(sm + L_TOP + 16);
            hi = t0.x; lo = t0.y; top2_merge(hi, lo, t0.z, t0.w); top2_merge(hi, lo, t1.x, t1.y); top2_merge(hi, lo, t1.z, t1.w);
        }
        hi = __builtin_amdgcn_readfirstlane(hi); lo = __builtin_amdgcn_readfirstlane(lo);
        const int sel0 = 2047 - (hi & 2047), sel1 = 2047 - (lo & 2047);
        TS(3);
        if (sub == 3) {
            if (lane == 0) { *ctl(sm, C_SEL0) = hi; *ctl(sm, C_SEL1) = lo; }
            lds_signal(sm, C_ROUTER, lane);
            if (b == 0 && lane == 0) __builtin_amdgcn_raw_buffer_store_b32(tag, rws, WS_EPOCH, 0, SC1);
        }
        if (sub < 2) {
            const int row = sub == 0 ? sel0 : sel1;
            u32x4 a[2];
#pragma unroll
            for (int h = 0; h < 2; ++h) a[h] = __builtin_amdgcn_raw_buffer_load_b128(rq, lane * 16 + h * 1024, row * 2048, 0);
            u32x2 sg;
            _Pragma("nounroll") for (int it = 0; it < SPIN_CAP; ++it) {
                asm volatile("" ::: "memory");
                sg = __builtin_amdgcn_raw_buffer_load_b64(rws, WS_SG + row * 8, 0, SC1);
                if (__builtin_amdgcn_readfirstlane(sg.y) == tag) break;
            }
            const float m = lo2f(sg.x), rs = __builtin_amdgcn_rcpf(hi2f(sg.x));
#pragma unroll
            for (int h = 0; h < 2; ++h) {
                u32x4 o;
#pragma unroll
                for (int e = 0; e < 4; ++e) { u32 d2 = pack2(lo2f(a[h][e]) - m, hi2f(a[h][e]) - m); o[e] = pack2(lo2f(d2) * rs, hi2f(d2) * rs); }
                *(LAS u32x4*)(sm + L_XS + sub * 2048 + h * 1024 + lane * 16) = o;
                if (h * 64 + lane == (rb >> 1)) *(LAS u32x2*)(sm + L_XOWN + sub * 8) = (rb & 1) ? (u32x2){a[h].z, a[h].w} : (u32x2){a[h].x, a[h].y};
            }
            lds_signal(sm, C_XSEL, lane);
        }
        TS(4);
        f32x2 xn[8][2], sx[8][2];
#pragma unroll
        for (int r = 0; r < 8; ++r) {
            const f32x2 sv = *(LAS f32x2*)(sm + L_STAT + r * 8);
#pragma unroll
            for (int h = 0; h < 2; ++h) {
                const float x0 = lo2f(xq[r][h]), x1 = hi2f(xq[r][h]);
                u32 d2 = pack2(x0 - sv.x, x1 - sv.x);
                u32 n2 = pack2(lo2f(d2) * sv.y, hi2f(d2) * sv.y);
                xn[r][h] = (f32x2){lo2f(n2), hi2f(n2)};
                sx[r][h] = xn[r][h] + (f32x2){x0, x1};
            }
        }
#pragma unroll
        for (int r = 0; r < 8; ++r) asm volatile("" : "+v"(xn[r][0]), "+v"(xn[r][1]), "+v"(sx[r][0]), "+v"(sx[r][1]));
        lds_wait(sm, C_Y, 4);
        TS(5);
        u32x4 y[4];
        _Pragma("nounroll") for (int it = 0; it < SPIN_CAP; ++it) {
            asm volatile("" ::: "memory");
#pragma unroll
            for (int i = 0; i < 4; ++i) y[i] = __builtin_amdgcn_raw_buffer_load_b128(rws, WS_Y + rb * 16384 + j * 64 + i * 16, 0, SC1);
            if (__all(umax(umax(max4(y[0]), max4(y[1])), umax(max4(y[2]), max4(y[3]))) != EMPTY)) break;
        }
        TS(6);
        const rsrc_t ro = mkrsrc(out, S * D * 2);
#pragma unroll
        for (int r = 0; r < 8; ++r) {
            u32x2 o;
#pragma unroll
            for (int h = 0; h < 2; ++h) {
                const u32 yy = y[r & 3][(r >> 2) * 2 + h];
                const f32x2 res = __builtin_elementwise_fma(xn[r][h], (f32x2){lo2f(yy), hi2f(yy)}, sx[r][h]);
                o[h] = pack2(res.x, res.y);
            }
            const int row = rb * 8 + r;
            if (row != sel0 && row != sel1) __builtin_amdgcn_raw_buffer_store_b64(o, ro, j * 8, row * 2048, 0);
        }
        TS(7);
    } else if (role == 0) {
        const int ch = rb * 4 + sub;
        const int par = lane >> 5, mm = (lane >> 2) & 7;
        const u32 yoff = WS_Y + (sub * 64 + 2 * mm + par) * 16384 + rb * 64 + (lane & 3) * 16;
#pragma unroll
        for (int m = 0; m < 4; ++m) __builtin_amdgcn_raw_buffer_store_b128(empty4, rclr, yoff + m * 16 * 16384, 0, SC1);
        PIN();
        KV a;
        kv_load(a, mkrsrc(k, S * D * 2), mkrsrc(v, S * D * 2), mkrsrc(shk, D * 8), rb, tid);
        PIN();
        const rsrc_t rh = mkrsrc(ssk + (size_t)ch * S, S * 2);
        u32x4 C[4], P[4];
#pragma unroll
        for (int m = 0; m < 4; ++m) {
            C[m] = __builtin_amdgcn_raw_buffer_load_b128(rh, lane * 16 + m * 1024, 0, POL_A);
            P[m] = __builtin_amdgcn_raw_buffer_load_b128(rh, lane * 16 + m * 1024 - 16, 0, POL_A);
        }
        const u32 dbits = ssD[ch];
        PIN();
        __builtin_amdgcn_s_barrier();
        PIN();
        u32x4 o0, o1;
        kv_compute(a, rb, o0, o1);
        asm volatile("" : "+v"(o0), "+v"(o1));
        TS(1);
        lds_signal(sm, C_CLEAR, lane);
        lds_wait(sm, C_CLEAR, 12);
        { const u32 off = kv_off(wave, lane, rb);
          __builtin_amdgcn_raw_buffer_store_b128(o0, rws, off, 0, SC1);
          __builtin_amdgcn_raw_buffer_store_b128(o1, rws, off + 16 * 16384, 0, SC1); }
        TS(2);
        LAS u32x4* U = (LAS u32x4*)(sm + sub * CH_BYTES);
        LAS u32x4* H = U + 288;
        const u32x4 zero = {0u, 0u, 0u, 0u};
        if (lane < 32) U[lane] = zero;
        if (lane < 8) H[hbase(lane)] = zero;
#pragma unroll
        for (int m = 0; m < 4; ++m) {
            const u32 W[8] = {P[m].x, P[m].y, P[m].z, P[m].w, C[m].x, C[m].y, C[m].z, C[m].w};
            u32 O[7];
#pragma unroll
            for (int e = 0; e < 7; ++e) O[e] = __builtin_amdgcn_alignbit(W[e + 1], W[e], 16);
#pragma unroll
            for (int s = 0; s < 8; ++s) {
                const int e = (s + 1) >> 1;
                H[hbase(s) + lane + 64 * m + 1] = (s & 1) ? (u32x4){W[e], W[e + 1], W[e + 2], W[e + 3]} : (u32x4){O[e], O[e + 1], O[e + 2], O[e + 3]};
            }
        }
        const int i = lane & 15, g = lane >> 4;
        u32x4 AD;
        { const int jj = 15 - i - 8 * g;
#pragma unroll
          for (int e = 0; e < 4; ++e) AD[e] = jj == 2 * e ? dbits : (jj == 2 * e + 1 ? dbits << 16 : 0u); }
        TS(3);
        lds_wait(sm, C_ROUTER, 1);
        TS(4);
        u32x4 uu[4];
        _Pragma("nounroll") for (int it = 0; it < SPIN_CAP; ++it) {
            asm volatile("" ::: "memory");
#pragma unroll
            for (int m = 0; m < 4; ++m) uu[m] = __builtin_amdgcn_raw_buffer_load_b128(rws, WS_KV + rb * 16384 + (m * 64 + lane) * 64 + sub * 16, 0, SC1);
            if (__all(umax(umax(max4(uu[0]), max4(uu[1])), umax(max4(uu[2]), max4(uu[3]))) != EMPTY)) break;
        }
        TS(5);
#pragma unroll
        for (int m = 0; m < 4; ++m) U[32 + m * 64 + lane] = uu[m];
        lds_signal(sm, C_KV, lane);
        __builtin_amdgcn_s_setprio(2);
        const LAS u32x4* Ap = H + hbase(i & 7) + (i >> 3) + g;
        const LAS u32x4* Bp = U + 32 + 2 * i + 1 - g;
        f32x4 acc[8];
#pragma unroll
        for (int n = 0; n < 8; ++n)
            acc[n] = __builtin_amdgcn_mfma_f32_16x16x32_bf16(__builtin_bit_cast(bf16x8, AD), __builtin_bit_cast(bf16x8, Bp[32 * n]), (f32x4){0.f, 0.f, 0.f, 0.f}, 0, 0, 0);
#pragma unroll 1
        for (int k0 = 0; k0 < 8; ++k0) {
            bf16x8 A[8], B[8];
#pragma unroll
            for (int m = 0; m < 8; ++m) {
                A[m] = __builtin_bit_cast(bf16x8, Ap[32 * m + 4 * k0]);
                B[m] = __builtin_bit_cast(bf16x8, Bp[32 * m - 4 * k0]);
            }
#pragma unroll
            for (int n = 0; n < 8; ++n)
#pragma unroll
                for (int k1 = 0; k1 <= n; ++k1)
                    acc[n] = __builtin_amdgcn_mfma_f32_16x16x32_bf16(A[k1], B[n - k1], acc[n], 0, 0, 0);
        }
        TS(6);
        LAS char* Y = (LAS char*)sm + U_BYTES;
#pragma unroll
        for (int n = 0; n < 8; ++n)
            *(LAS u32x2*)(Y + sub * (CH_BYTES + 64) + n * 512 + lane * 8) = (u32x2){pack2(acc[n][0], acc[n][1]), pack2(acc[n][2], acc[n][3])};
        lds_signal(sm, C_Y, lane);
        lds_wait(sm, C_Y, 4);
        {
            const int qq = (lane >> 2) & 3, p = lane & 3, grp = (lane >> 4) & 1;
            LAS char* base = Y + qq * (CH_BYTES + 64) + sub * 1024 + par * 256 + (grp * 4 + p) * 8;
#pragma unroll
            for (int m = 0; m < 4; ++m) {
                LAS char* pa = base + (m >> 1) * 512 + (m & 1) * 64;
                const u32x2 r0 = __builtin_bit_cast(u32x2, __builtin_amdgcn_ds_read_tr16_b64_v4i16((LAS s16x4*)pa));
                const u32x2 r1 = __builtin_bit_cast(u32x2, __builtin_amdgcn_ds_read_tr16_b64_v4i16((LAS s16x4*)(pa + 128)));
                __builtin_amdgcn_raw_buffer_store_b128((u32x4){noempty(r0.x), noempty(r0.y), noempty(r1.x), noempty(r1.y)}, rws, yoff + m * 16 * 16384, 0, SC1);
            }
        }
        TS(7);
    } else {
        const bool has = (b >> 3) < 28;
        const int fs = has ? (b & 7) * 28 + (b >> 3) : 0;
        const int n = lane & 15, g = lane >> 4;
        const rsrc_t rp = mkrsrc(ws, has ? WS_BYTES : 0u);
        const u32 poff = WS_P + (sub * 32 + n) * (224 * 64) + fs * 64 + g * 16;
#pragma unroll
        for (int be = 0; be < 2; ++be) __builtin_amdgcn_raw_buffer_store_b128(empty4, (CLRMASK & 2) ? rp : rclr, poff + be * 16 * (224 * 64), 0, SC1);
        PIN();
        KV a;
        kv_load(a, mkrsrc(k, S * D * 2), mkrsrc(v, S * D * 2), mkrsrc(shk, D * 8), rb, tid);
        PIN();
        u32x4 G[2][8], Wd[2][4];
        {
            const bool hasw = has;
            const rsrc_t rd = mkrsrc(wd, hasw ? D * F * 2 : 0);
            const u32 rstride = hasw ? F * 2 : 0;
            const char* gp = (const char*)((n & 2) ? wu : wg) + (sub * 256 + ((n >> 2) * 4 + g) * 8) * rstride + fs * 32 + (n & 1) * 16;
#pragma unroll
            for (int be = 0; be < 2; ++be)
#pragma unroll
                for (int jj = 0; jj < 8; ++jj) G[be][jj] = POL_W ? __builtin_nontemporal_load((const u32x4*)(gp + (be * 128 + jj) * rstride)) : *(const u32x4*)(gp + (be * 128 + jj) * rstride);
#pragma unroll
            for (int be = 0; be < 2; ++be)
#pragma unroll
                for (int jj = 0; jj < 4; ++jj)
                    Wd[be][jj] = __builtin_amdgcn_raw_buffer_load_b128(rd, (fs * 16 + 4 * g + jj) * 2048 + (sub * 2 + be) * 256 + n * 16, 0, POL_W);
        }
        PIN();
        __builtin_amdgcn_s_barrier();
        PIN();
        u32x4 o0, o1;
        kv_compute(a, rb, o0, o1);
        asm volatile("" : "+v"(o0), "+v"(o1));
        TS(1);
        lds_signal(sm, C_CLEAR, lane);
        lds_wait(sm, C_CLEAR, 12);
        { const u32 off = kv_off(wave, lane, rb);
          __builtin_amdgcn_raw_buffer_store_b128(o0, rws, off, 0, SC1);
          __builtin_amdgcn_raw_buffer_store_b128(o1, rws, off + 16 * 16384, 0, SC1); }
        TS(2);
        u32x4 GB[2][8]; u32x2 DB[2][8];
#pragma unroll
        for (int be = 0; be < 2; ++be)
#pragma unroll
            for (int e = 0; e < 4; ++e) {
#pragma unroll
                for (int pr = 0; pr < 4; ++pr) {
                    GB[be][2 * e][pr] = __builtin_amdgcn_perm(G[be][2 * pr + 1][e], G[be][2 * pr][e], 0x05040100u);
                    GB[be][2 * e + 1][pr] = __builtin_amdgcn_perm(G[be][2 * pr + 1][e], G[be][2 * pr][e], 0x07060302u);
                }
            }
#pragma unroll
        for (int be = 0; be < 2; ++be)
#pragma unroll
            for (int c = 0; c < 8; ++c) asm volatile("" : "+v"(GB[be][c]));
        TS(3);
        lds_wait(sm, C_XSEL, 2);
        TS(4);
        f32x4 ga[4];
#pragma unroll
        for (int c = 0; c < 4; ++c) ga[c] = (f32x4){0.f, 0.f, 0.f, 0.f};
#pragma unroll
        for (int be = 0; be < 2; ++be) {
            const u32x4 xa = *(LAS u32x4*)(sm + L_XS + (n & 1) * 2048 + (sub * 256 + be * 128 + ((n >> 2) * 4 + g) * 8) * 2);
            const u32x4 z4 = {0u, 0u, 0u, 0u};
            const u32x4 xe = (n & 2) ? z4 : xa, xo = (n & 2) ? xa : z4;
#pragma unroll
            for (int c = 0; c < 8; ++c)
                ga[c >> 1] = __builtin_amdgcn_mfma_f32_16x16x32_bf16(__builtin_bit_cast(bf16x8, (c & 1) ? xo : xe), __builtin_bit_cast(bf16x8, GB[be][c]), ga[c >> 1], 0, 0, 0);
        }
        if (g == (n >> 2)) {
#pragma unroll
            for (int c = 0; c < 4; ++c) *(LAS f32x4*)(sm + L_RED + (sub * 4 + g) * 256 + (n & 3) * 64 + c * 16) = ga[c];
        }
        lds_signal(sm, C_F, lane);
#pragma unroll
        for (int be = 0; be < 2; ++be)
#pragma unroll
            for (int e = 0; e < 4; ++e)
#pragma unroll
                for (int pr = 0; pr < 2; ++pr) {
                    DB[be][2 * e][pr] = __builtin_amdgcn_perm(Wd[be][2 * pr + 1][e], Wd[be][2 * pr][e], 0x05040100u);
                    DB[be][2 * e + 1][pr] = __builtin_amdgcn_perm(Wd[be][2 * pr + 1][e], Wd[be][2 * pr][e], 0x07060302u);
                }
#pragma unroll
        for (int be = 0; be < 2; ++be)
#pragma unroll
            for (int c = 0; c < 8; ++c) asm volatile("" : "+v"(DB[be][c]));
        lds_wait(sm, C_F, 4);
        {
            float s = 0;
#pragma unroll
            for (int p = 0; p < 16; ++p) s += *(LAS float*)(sm + L_RED + p * 256 + lane * 4);
            s = rbf(s);
            auto sw = __builtin_amdgcn_permlane32_swap(__float_as_uint(s), __float_as_uint(s), false, false);
            const float gg = __uint_as_float(sw[0]), up = __uint_as_float(sw[1]);
            const float hv = rbf(gg * __builtin_amdgcn_rcpf(1.0f + __expf(-gg))) * up;
            if (lane < 32) *(LAS u16*)(sm + L_HB + sub * 64 + (lane & 1) * 32 + (lane >> 1) * 2) = (u16)pack2(hv, 0.f);
        }
        const u32x2 ha = *(LAS u32x2*)(sm + L_HB + sub * 64 + (n & 1) * 32 + g * 8);
#pragma unroll
        for (int be = 0; be < 2; ++be) {
            f32x4 da = {0.f, 0.f, 0.f, 0.f};
#pragma unroll
            for (int c = 0; c < 8; ++c) {
                const u32x2 ac = (n >> 1) == c ? ha : (u32x2){0u, 0u};
                da = __builtin_amdgcn_mfma_f32_16x16x16bf16_1k(__builtin_bit_cast(s16x4, ac), __builtin_bit_cast(s16x4, DB[be][c]), da, 0, 0, 0);
            }
            __builtin_amdgcn_raw_buffer_store_b128((u32x4){noempty(__float_as_uint(da[0])), noempty(__float_as_uint(da[1])), noempty(__float_as_uint(da[2])), noempty(__float_as_uint(da[3]))},
                                                   rp, poff + be * 16 * (224 * 64), 0, SC1);
        }
        TS(5);
        if (sub == 0) {
            lds_wait(sm, C_KV, 1);
            u32x4 pp[7];
            _Pragma("nounroll") for (int it = 0; it < SPIN_CAP; ++it) {
                asm volatile("" ::: "memory");
                u32 mx = 0;
#pragma unroll
                for (int m = 0; m < 7; ++m) {
                    pp[m] = __builtin_amdgcn_raw_buffer_load_b128(rws, WS_P + (rb >> 1) * (224 * 64) + (m * 32 + (lane >> 1)) * 64 + (rb & 1) * 32 + (lane & 1) * 16, 0, SC1);
                    mx = umax(mx, max4(pp[m]));
                }
                if (__all(mx != EMPTY)) break;
                __builtin_amdgcn_s_sleep(2);
            }
            TS(6);
            float sv[4];
#pragma unroll
            for (int e = 0; e < 4; ++e) {
                float s = 0;
#pragma unroll
                for (int m = 0; m < 7; ++m) s += __uint_as_float(pp[m][e]);
                s += dppf<0x4E>(s); s += dppf<0x124>(s); s += dppf<0x128>(s);
                sv[e] = xrow_sum(s);
            }
            const int e = (lane >> 1) & 3, tok = e & 1, c = 2 * (lane & 1) + (e >> 1);
            const float yv = rbf(e == 0 ? sv[0] : (e == 1 ? sv[1] : (e == 2 ? sv[2] : sv[3])));
            const u32 kq = *ctl(sm, C_SEL0 + tok);
            const int row = 2047 - (kq & 2047);
            const u32 kb = kq >> 16;
            const float rv = lo2f((kb & 0x8000u) ? (kb & 0x7fffu) : (~kb & 0xffffu));
            const float upd = rbf(yv * rbf(__builtin_amdgcn_rcpf(1.0f + __expf(-rv))));
            const float x = lo2f(*(LAS u16*)(sm + L_XOWN + tok * 8 + c * 2));
            const float xn = lo2f(*(LAS u16*)(sm + L_XS + tok * 2048 + (rb * 4 + c) * 2));
            lds_wait(sm, C_Y, 4);
            const float y2 = lo2f(*(LAS u16*)(sm + U_BYTES + c * (CH_BYTES + 64) + (row >> 8) * 512 + (((row >> 2) & 3) * 16 + ((row >> 4) & 15)) * 8 + (row & 3) * 2));
            const float res = rbf(x + rbf(xn * y2)) + rbf(xn + upd);
            if (lane < 8) out[(size_t)row * D + rb * 4 + c] = (u16)pack2(res, 0.f);
            TS(7);
        }
    }
    TS_OUT;
}

extern "C" void kernel_launch(void* const* d_in, const int* in_sizes, int n_in, void* d_out, int out_size, void* d_ws, size_t ws_size,
                              hipStream_t stream) {
    (void)hipFuncSetAttribute((const void*)fused_kernel, hipFuncAttributeMaxDynamicSharedMemorySize, L_END);
    fused_kernel<<<256, 768, L_END, stream>>>((const u16*)d_in[0], (const u16*)d_in[1], (const u16*)d_in[2], (const u16*)d_in[3], (const u16*)d_in[4],
                                              (const u16*)d_in[5], (const u16*)d_in[7], (const u16*)d_in[8], (const u16*)d_in[9], (const u16*)d_in[10],
                                              (u16*)d_out, (char*)d_ws);
}
```

```cpp
#include <hip/hip_runtime.h>
#include <stdint.h>

typedef unsigned short u16;
typedef unsigned int u32;
typedef unsigned long long u64;
typedef __attribute__((ext_vector_type(8))) __bf16 bf16x8;
typedef __attribute__((ext_vector_type(4))) float f32x4;
typedef __attribute__((ext_vector_type(2))) float f32x2;
typedef __attribute__((ext_vector_type(4))) u32 u32x4;
typedef __attribute__((ext_vector_type(2))) u32 u32x2;
typedef __attribute__((ext_vector_type(2))) __bf16 bf16x2;
#define LAS __attribute__((address_space(3)))
typedef __amdgpu_buffer_rsrc_t rsrc_t;

constexpr int S = 2048, D = 1024, F = 3584;
constexpr int SC1 = 16, NT = 2;
constexpr u32 EMPTY = 0xFFFFFFFFu;
constexpr u32 OOB = 0x80000000u;
constexpr int SPIN_CAP = 1 << 20;
constexpr int CODE_BYTES = 17664;
constexpr int NREP = 8;
constexpr int GA = 3, GB = 7, T0AT = 13, WIN = 5, WDWIN = 3;
constexpr int PDELAY = 20, YDELAY = 10, HDELAY = 8, HDELAY2 = 8;

constexpr u32 WS_EPOCH = 0, WS_RG = 64u << 10  , WS_SG = 128u << 10  , WS_FL = 512u << 10  ,
              WS_A = 768u << 10  , WS_KV = 1u << 20  , WS_P = 5u << 20  , WS_BYTES = 6u << 20;
constexpr int U_BYTES = 288 * 16, HC_BYTES = 4160, CH_BYTES = U_BYTES + 4 * HC_BYTES + 32;
constexpr int L_RED = 4 * CH_BYTES, L_XS = L_RED + 4096, L_Y0 = L_XS + 1536, L_OLD = L_Y0 + 16512, L_CTL = L_OLD + 16 * 1040, L_TOP = L_CTL + 128, L_HB = L_TOP + 96, L_END = L_HB + 256;
static_assert(L_END <= 163840 && L_END > 81920 && L_Y0 % 128 == 0, "LDS map: one workgroup per CU");
enum { C_ARR = 0, C_RDY = 4, C_SEL0 = 8, C_SEL1 = 9, C_E = 10, C_ROUTER = 11, C_HB = 12, C_F = 13, C_Y = 14  , C_KV = 16  ,
       C_TAG = 18, C_TAGOK = 19, C_XSEL = 20, C_ISSUED = 21, C_KST = 22, C_ST0 = 23, C_WALL = 25, C_PUB = 26, C_YS = 29   };

__device__ __forceinline__ float lo2f(u32 v) { return __uint_as_float(v << 16); }
__device__ __forceinline__ float hi2f(u32 v) { return __uint_as_float(v & 0xffff0000u); }
__device__ __forceinline__ u32 sel(bool c, u32 a, u32 b) { return c ? a : b; }
__device__ __forceinline__ float self(bool c, float a, float b) { return c ? a : b; }
__device__ __forceinline__ int opaque_s(int x) { asm volatile("" : "+s"(x)); return x; }
__device__ __forceinline__ float opaque_zero() { float z = 0.f; asm("" : "+s"(z)); return z; }
__device__ __forceinline__ u32 pack2v(f32x2 v) { return __builtin_bit_cast(u32, __builtin_convertvector(v, bf16x2)); }
__device__ __forceinline__ u32 pack2(float lo, float hi) { return pack2v((f32x2){lo, hi}); }
__device__ __forceinline__ float rbf(float f) { return __uint_as_float(pack2v((f32x2){opaque_zero(), f})); }
__device__ __forceinline__ f32x2 rbf2(f32x2 v) { return (f32x2){rbf(v.x), rbf(v.y)}; }
__device__ __forceinline__ f32x2 unpk(u32 v) { return (f32x2){lo2f(v), hi2f(v)}; }
__device__ __forceinline__ u32 umin(u32 a, u32 b) { return a < b ? a : b; }
__device__ __forceinline__ u32 umax(u32 a, u32 b) { return a > b ? a : b; }
__device__ __forceinline__ u32x4 noempty(f32x4 a) {
    return (u32x4){umin(__float_as_uint(a[0]), EMPTY - 1u), umin(__float_as_uint(a[1]), EMPTY - 1u), umin(__float_as_uint(a[2]), EMPTY - 1u), umin(__float_as_uint(a[3]), EMPTY - 1u)};
}
__device__ __forceinline__ u32 max4(u32x4 a) { return umax(umax(a.x, a.y), umax(a.z, a.w)); }
__device__ __forceinline__ rsrc_t mkrsrc(const void* p, u32 bytes) { return __builtin_amdgcn_make_buffer_rsrc((void*)p, 0, bytes, 0x00020000); }
__device__ __forceinline__ u32x4 srd4(const void* p) { return (u32x4){(u32)(size_t)p, (u32)((size_t)p >> 32) & 0xffffu, WS_BYTES, 0x00020000u}; }
template <int CTRL> __device__ __forceinline__ u32 dppu(u32 v) { return (u32)__builtin_amdgcn_update_dpp(0, (int)v, CTRL, 0xf, 0xf, true); }
template <int CTRL> __device__ __forceinline__ float dppf(float v) { return __uint_as_float(dppu<CTRL>(__float_as_uint(v))); }
__device__ __forceinline__ float xrow_sum(float v) {
    auto a = __builtin_amdgcn_permlane16_swap(__float_as_uint(v), __float_as_uint(v), false, false);
    v = __uint_as_float(a[0]) + __uint_as_float(a[1]);
    auto b = __builtin_amdgcn_permlane32_swap(__float_as_uint(v), __float_as_uint(v), false, false);
    return __uint_as_float(b[0]) + __uint_as_float(b[1]);
}
__device__ __forceinline__ void top2_merge(u32& hi, u32& lo, u32 ohi, u32 olo) {
    u32 nlo = umax(umin(hi, ohi), umax(lo, olo));
    hi = umax(hi, ohi); lo = nlo;
}

#define LDS(type, off) (*(LAS type*)(size_t)(u32)(off))
__device__ __forceinline__ void lds_signal(int i) {
    asm volatile("s_mov_b64 exec, 1\n\tds_add_u32 %0, %1\n\ts_mov_b64 exec, -1" :: "v"(L_CTL + i * 4), "v"(1u) : "memory");
}
__device__ __forceinline__ u32 lds_arrive(int i) {
    u32 old;
    asm volatile("s_mov_b64 exec, 1\n\tds_add_rtn_u32 %0, %1, %2\n\ts_mov_b64 exec, -1\n\ts_waitcnt lgkmcnt(0)" : "=&v"(old) : "v"(L_CTL + i * 4), "v"(1u) : "memory");
    return __builtin_amdgcn_readfirstlane(old);
}
__device__ __forceinline__ void lds_wait(int i, u32 want) {
    u32 t, c, n;
    asm volatile("s_mov_b32 %2, 0x1000000\n"
                 "1:\n\tds_read_b32 %0, %3\n\ts_waitcnt lgkmcnt(0)\n\tv_readfirstlane_b32 %1, %0\n\ts_cmp_ge_u32 %1, %4\n\ts_cbranch_scc1 2f\n\t"
                 "s_sleep 1\n\ts_sub_u32 %2, %2, 1\n\ts_cbranch_scc0 1b\n2:"
                 : "=&v"(t), "=&s"(c), "=&s"(n) : "v"(L_CTL + i * 4), "s"(want) : "memory", "scc");
}
#define PIN() do { asm volatile("" ::: "memory"); __builtin_amdgcn_sched_barrier(0); } while (0)
#define VMWAIT(n) do { PIN(); asm volatile("s_waitcnt vmcnt(%0)" :: "i"(n) : "memory"); PIN(); } while (0)
#define SPIN _Pragma("nounroll") for (int it = 0; it < SPIN_CAP; ++it)

struct KV { u32 k[11], v[8]; };
__device__ __forceinline__ void kv_compute(const KV& a, u32x4 sh, u32x4& o0, u32x4& o1) {
    f32x2 kk[11], ks[8];
#pragma unroll
    for (int r = 0; r < 11; ++r) kk[r] = unpk(a.k[r]);
    const f32x2 s0 = {lo2f(sh.x), lo2f(sh.z)}, s1 = {hi2f(sh.x), hi2f(sh.z)}, s2 = {lo2f(sh.y), lo2f(sh.w)}, s3 = {hi2f(sh.y), hi2f(sh.w)};
#pragma unroll
    for (int r = 0; r < 8; ++r) ks[r] = rbf2(kk[r] * s3);
#pragma unroll
    for (int r = 0; r < 8; ++r) ks[r] = rbf2(ks[r] + rbf2(kk[r + 1] * s2));
#pragma unroll
    for (int r = 0; r < 8; ++r) ks[r] = rbf2(ks[r] + rbf2(kk[r + 2] * s1));
#pragma unroll
    for (int r = 0; r < 8; ++r) ks[r] = rbf2(ks[r] + rbf2(kk[r + 3] * s0));
#pragma unroll
    for (int r = 0; r < 8; r += 2) asm volatile("" : "+v"(ks[r]), "+v"(ks[r + 1]));
    PIN();
#pragma unroll
    for (int r = 0; r < 8; ++r) ks[r] *= unpk(a.v[r]);
#pragma unroll
    for (int e = 0; e < 4; ++e) {
        auto sw = __builtin_amdgcn_permlane16_swap(pack2(ks[7 - 2 * e].x, ks[6 - 2 * e].x), pack2(ks[7 - 2 * e].y, ks[6 - 2 * e].y), false, false);
        o0[e] = sw[0]; o1[e] = sw[1];
    }
}
__device__ __forceinline__ void h_stage(const u32x4 (&hc)[4], int hw) {
    u32 pz = 0, pw = 0;
#pragma unroll
    for (int m = 0; m < 4; ++m) {
        const u32 w2 = (u32)__builtin_amdgcn_update_dpp((int)dppu<0x13C>(pz), (int)hc[m].z, 0x138, 0xf, 0xf, false);
        const u32 w3 = (u32)__builtin_amdgcn_update_dpp((int)dppu<0x13C>(pw), (int)hc[m].w, 0x138, 0xf, 0xf, false);
        pz = hc[m].z; pw = hc[m].w;
        const u32 o2 = __builtin_amdgcn_alignbit(w3, w2, 16), o3 = __builtin_amdgcn_alignbit(hc[m].x, w3, 16), o4 = __builtin_amdgcn_alignbit(hc[m].y, hc[m].x, 16),
                  o5 = __builtin_amdgcn_alignbit(hc[m].z, hc[m].y, 16), o6 = __builtin_amdgcn_alignbit(hc[m].w, hc[m].z, 16);
        LDS(u32x4, hw + m * 1024 + 3 * HC_BYTES) = hc[m];
        LDS(u32x4, hw + m * 1024) = (u32x4){o2, o3, o4, o5};
        LDS(u32x4, hw + m * 1024 + HC_BYTES) = (u32x4){w3, hc[m].x, hc[m].y, hc[m].z};
        LDS(u32x4, hw + m * 1024 + 2 * HC_BYTES) = (u32x4){o3, o4, o5, o6};
    }
}
__device__ __forceinline__ u32 get_tag() { lds_wait(C_TAGOK, 1); return __builtin_amdgcn_readfirstlane(LDS(u32, L_CTL + C_TAG * 4)); }
__device__ __forceinline__ void kv_post(rsrc_t rws, int cg, int lane, u32 tag) {
    const bool last = lds_arrive(C_ARR) == 7;
    __builtin_amdgcn_raw_buffer_store_b32(tag, rws, sel(last && lane == 0, WS_FL + cg * 128, OOB), 0, SC1);
    PIN();
}
__device__ __forceinline__ void flag_poll(const char* ws, u32 fo, u32 tag, int rdy) {
    u32 f0, f1, f2, n = SPIN_CAP;
#define PL(r) "buffer_load_dword " r ", %4, %5, 0 offen sc1\n\ts_sleep 4\n\t"
#define CHK(r) "s_waitcnt vmcnt(2)\n\tv_cmp_ne_u32 vcc, %6, " r "\n\ts_cbranch_vccz 2f\n\t"
    asm volatile("s_nop 4\n\t" PL("%0") PL("%1")
                 "1:\n\t" PL("%2") CHK("%0") PL("%0") CHK("%1") PL("%1") CHK("%2")
                 "s_sub_u32 %3, %3, 1\n\ts_cbranch_scc0 1b\n"
                 "2:\n\ts_mov_b64 exec, 1\n\tds_add_u32 %7, %8\n\ts_mov_b64 exec, -1\n\ts_waitcnt vmcnt(0)"
                 : "=&v"(f0), "=&v"(f1), "=&v"(f2), "+s"(n)
                 : "v"(fo), "s"(srd4(ws)), "s"(tag), "v"(L_CTL + rdy * 4), "v"(1u)
                 : "memory", "scc", "vcc");
}
__device__ __forceinline__ int ybase(int c) { return L_Y0 + c * 4096 + (c & 1) * 8 + (c >> 1) * 64; }
__device__ __forceinline__ void tr8(u32x4 (&w)[8]) {
    u32x4 t[8];
#pragma unroll
    for (int pr = 0; pr < 4; ++pr) {
#pragma unroll
        for (int c = 0; c < 8; ++c) t[c][pr] = __builtin_amdgcn_perm(w[2 * pr + 1][c >> 1], w[2 * pr][c >> 1], (c & 1) ? 0x07060302u : 0x05040100u);
        __builtin_amdgcn_sched_barrier(0);
    }
#pragma unroll
    for (int c = 0; c < 8; ++c) { w[c] = t[c]; asm volatile("" : "+v"(w[c])); }
}
__device__ __forceinline__ void mm8(f32x4 (&acc)[8], u32x4 a, const u32x4 (&w)[8]) {
#pragma unroll
    for (int c = 0; c < 8; ++c) acc[c] = __builtin_amdgcn_mfma_f32_16x16x32_bf16(__builtin_bit_cast(bf16x8, a), __builtin_bit_cast(bf16x8, w[c]), acc[c], 0, 0, 0);
}
__device__ __forceinline__ f32x4 pick(const f32x4 (&acc)[8], int g, int o) {
    f32x4 r;
#pragma unroll
    for (int e = 0; e < 4; ++e)
        r[e] = self(g & 2, self(g & 1, acc[6 + (e >> 1)][o + (e & 1)], acc[4 + (e >> 1)][o + (e & 1)]), self(g & 1, acc[2 + (e >> 1)][o + (e & 1)], acc[e >> 1][o + (e & 1)]));
    return r;
}

__global__ void __launch_bounds__(768, 3)
fused_kernel(const u16* q, const u16* k, const u16* v, const u16* shk, const u16* ssk, const u16* ssD, const u16* wr, const u16* wg,
             const u16* wu, const u16* wd, u16* out, char* ws) {
    const int tid = threadIdx.x, lane = tid & 63;
    const int wave = __builtin_amdgcn_readfirstlane(tid >> 6), sub = wave & 3;
    const int b = blockIdx.x, gj = b & 7, gi = b >> 3;
    const int G = gj * 2 + (gi & 1), I = gi >> 1, cg = G * 16 + I;
    const int wf = gj * 28 + gi;
    const int i = lane & 15, g = lane >> 4;
    {
        u64 pc;
        asm volatile("s_getpc_b64 %0\n\tv_cmp_gt_u32 vcc, %2, %1\n\ts_and_b64 exec, vcc, exec\n\ts_cbranch_execz 1f\n\t"
                     "global_load_dword v167, %1, %0\n1:\n\ts_mov_b64 exec, -1"
                     : "=&s"(pc) : "v"(tid * 128), "s"(CODE_BYTES) : "vcc", "scc", "v167", "memory");
    }
    PIN();
    asm volatile("ds_write_b32 %0, %1" :: "v"(L_CTL + (lane & 31) * 4), "v"(0) : "memory");
    KV a; u32x4 sh;
    const int co = G * 128 + (lane & 31) * 4;
    const u32 vo = (I * 128 + wave * 16 + (lane >> 5) * 8) * 2048 + co;
    if (wave < 8) {
        const rsrc_t rk = mkrsrc(k, S * D * 2);
        PIN();
#pragma unroll
        for (int r = 3; r < 11; ++r) a.k[r] = __builtin_amdgcn_raw_buffer_load_b32(rk, vo, (r - 3) * 2048, 0);
        const u32 vh = sel(vo < 2048, OOB, vo - 3 * 2048);
#pragma unroll
        for (int r = 0; r < 3; ++r) a.k[r] = __builtin_amdgcn_raw_buffer_load_b32(rk, vh, r * 2048, 0);
        sh = __builtin_amdgcn_raw_buffer_load_b128(mkrsrc(shk, D * 8), co * 4, 0, 0);
    }
    PIN();
    asm volatile("s_waitcnt lgkmcnt(0)\n\ts_barrier" ::: "memory");
    PIN();
    if (opaque_s(wave) >= 8) {
        __builtin_amdgcn_s_waitcnt(0);
        asm volatile("" : "+s"(ws));
        const rsrc_t rws = mkrsrc(ws, WS_BYTES), rq = mkrsrc(q, S * D * 2);
        const u32 sgb = WS_SG + gj * 16384;
        __builtin_amdgcn_s_setprio(3);
        u32 epoch = __builtin_amdgcn_raw_buffer_load_b32(rws, sel(sub == 0 && lane == 0, WS_EPOCH, OOB), 0, SC1);
        u32 wrm = __builtin_amdgcn_raw_buffer_load_b32(rws, sel(sub == 1 && lane == 0, WS_FL + cg * 128,
                                                               sel(sub == 2 && lane == 0, WS_RG + b * 128, sel(sub == 3 && lane < 4, WS_SG + b * 512 + lane * 128, OOB))), 0, SC1);
        PIN();
        __builtin_amdgcn_raw_buffer_store_b128((u32x4){EMPTY, EMPTY, EMPTY, EMPTY}, mkrsrc(ws, gi < 28 ? WS_BYTES : 0u),
                                               sel(lane < 32, WS_P + (((wf & 3) * 16 + sub * 4 + (lane >> 3)) * 56 + (wf >> 2)) * 128 + (lane & 7) * 16,
                                                   sel(lane < 48, WS_A + wf * 1024 + sub * 256 + i * 16, OOB)), 0, SC1);
        PIN();
        u32x4 qa[2][2], wa[2];
#pragma unroll
        for (int h = 0; h < 2; ++h) wa[h] = *(const u32x4*)(wr + h * 512 + lane * 8);
        PIN();
        lds_wait(C_ST0, 8);
#pragma unroll
        for (int r = 0; r < 2; ++r)
#pragma unroll
            for (int h = 0; h < 2; ++h) qa[r][h] = __builtin_amdgcn_raw_buffer_load_b128(rq, lane * 16 + h * 1024, (b * 8 + sub + 4 * r) * 2048, 0);
        PIN();
        __builtin_amdgcn_s_setprio(1);
        if (sub == 0) {
            asm volatile("" : "+v"(epoch));
            LDS(u32, L_CTL + C_TAG * 4) = __builtin_amdgcn_readfirstlane(epoch) * 1664525u + 1013904223u;
            lds_signal(C_TAGOK);
        }
        PIN();
        const u32 tag = get_tag();
        u32 xro = (wf & 3) * 512 + (lane & 31) * 16;
        LDS(u32x2, L_XS + 1024 + lane * 8) = (u32x2){0x3f803f80u, 0x3f803f80u};
        u32 rec = sel((lane & 7) < 5, sel((lane & 7) == 0, WS_RG + (lane >> 3) * 4096 + b * 16, WS_SG + (lane >> 3) * 16384 + b * 64 + (lane & 7) * 16 - 16), OOB);
        {
            f32x2 w2[8];
#pragma unroll
            for (int e = 0; e < 8; ++e) w2[e] = unpk(wa[e >> 2][e & 3]);
            float sw;
            { const f32x2 u = ((w2[0] + w2[1]) + (w2[2] + w2[3])) + ((w2[4] + w2[5]) + (w2[6] + w2[7])); sw = u.x + u.y; }
            sw += dppf<0xB1>(sw); sw += dppf<0x4E>(sw); sw += dppf<0x124>(sw); sw += dppf<0x128>(sw); sw = xrow_sum(sw);
            asm volatile("" : "+v"(sw), "+v"(rec), "+v"(xro));
            PIN();
            float t[2][3], x0[2];
#pragma unroll
            for (int r = 0; r < 2; ++r) {
                x0[r] = lo2f(__builtin_amdgcn_readfirstlane(qa[r][0][0]));
                f32x2 s1 = {0.f, 0.f}, s2 = {0.f, 0.f}, s3 = {0.f, 0.f};
#pragma unroll
                for (int e = 0; e < 8; ++e) {
                    const f32x2 d = unpk(qa[r][e >> 2][e & 3]) - x0[r];
                    s1 += d; s2 = __builtin_elementwise_fma(d, d, s2); s3 = __builtin_elementwise_fma(d, w2[e], s3);
                }
                t[r][0] = s1.x + s1.y; t[r][1] = s2.x + s2.y; t[r][2] = s3.x + s3.y;
            }
            PIN();
            float u[3];
#pragma unroll
            for (int z = 0; z < 3; ++z) u[z] = self(lane & 1, t[1][z], t[0][z]) + dppf<0xB1>(self(lane & 1, t[0][z], t[1][z]));
#pragma unroll
            for (int z = 0; z < 3; ++z) u[z] += dppf<0x4E>(u[z]);
#pragma unroll
            for (int z = 0; z < 3; ++z) u[z] += dppf<0x124>(u[z]);
#pragma unroll
            for (int z = 0; z < 3; ++z) u[z] += dppf<0x128>(u[z]);
#pragma unroll
            for (int z = 0; z < 3; ++z) u[z] = xrow_sum(u[z]);
            const float xs = self(lane & 1, x0[1], x0[0]), md = u[0] * (1.0f / D), m = rbf(xs + md);
            const float sd = rbf(__builtin_amdgcn_sqrtf(rbf(rbf(fmaxf(fmaf(-md, md, u[1] * (1.0f / D)), 0.f)) + 1e-5f)));
            const float sc = fmaf(xs - m, sw, u[2]) * __builtin_amdgcn_rcpf(sd);
            const u32 sb = __float_as_uint(rbf(sc));
            const u32 key = sel(sc == sc, sb ^ (((int)sb >> 31 | 0x80000000u) & 0xffff0000u), 0u) | (2047u - (b * 8 + sub + 4 * (lane & 1)));
            const u32 ko = dppu<0xB1>(key);
            LDS(u32, L_TOP + sub * 8 + (lane & 1) * 4) = sel(lane & 1, umin(key, ko), umax(key, ko));
            LDS(u32, L_TOP + 32 + (sub + 4 * (lane & 1)) * 4) = (__float_as_uint(m) >> 16) | __float_as_uint(sd);
        }
        asm volatile("" :: "v"(wrm));
        VMWAIT(0);
        const u32 ord = lds_arrive(C_E);
        if (ord == 3) {
            __builtin_amdgcn_s_setprio(3);
            u32 hi, lo;
            {
                const u32x4 t0 = LDS(u32x4, L_TOP), t1 = LDS(u32x4, L_TOP + 16);
                hi = t0.x; lo = t0.y; top2_merge(hi, lo, t0.z, t0.w); top2_merge(hi, lo, t1.x, t1.y); top2_merge(hi, lo, t1.z, t1.w);
            }
            {
                const u32x2 st = LDS(u32x2, L_TOP + 32 + ((lane - 1) & 3) * 8);
                const bool kl = (lane & 7) == 0;
                __builtin_amdgcn_raw_buffer_store_b128((u32x4){sel(kl, hi, st.x), tag, sel(kl, lo, st.y), tag}, rws, rec, 0, SC1);
            }
            PIN();
            lds_signal(C_PUB);
            lds_wait(C_RDY, 1);
            u32x4 t[4];
            SPIN {
                asm volatile("" ::: "memory");
                u32 bad = 0;
#pragma unroll
                for (int e = 0; e < 4; ++e) { t[e] = __builtin_amdgcn_raw_buffer_load_b128(rws, WS_RG + gj * 4096 + lane * 16 + e * 1024, 0, SC1); bad |= (t[e].y ^ tag) | (t[e].w ^ tag); }
                if (__all(bad == 0)) break;
                __builtin_amdgcn_s_sleep(2);
            }
            hi = t[0].x; lo = t[0].z;
#pragma unroll
            for (int e = 1; e < 4; ++e) top2_merge(hi, lo, t[e].x, t[e].z);
            top2_merge(hi, lo, dppu<0xB1>(hi), dppu<0xB1>(lo));
            top2_merge(hi, lo, dppu<0x4E>(hi), dppu<0x4E>(lo));
            top2_merge(hi, lo, dppu<0x141>(hi), dppu<0x141>(lo));
            top2_merge(hi, lo, dppu<0x140>(hi), dppu<0x140>(lo));
            { auto u = __builtin_amdgcn_permlane16_swap(hi, hi, false, false); auto c = __builtin_amdgcn_permlane16_swap(lo, lo, false, false);
              hi = u[0]; lo = c[0]; top2_merge(hi, lo, u[1], c[1]); }
            { auto u = __builtin_amdgcn_permlane32_swap(hi, hi, false, false); auto c = __builtin_amdgcn_permlane32_swap(lo, lo, false, false);
              hi = u[0]; lo = c[0]; top2_merge(hi, lo, u[1], c[1]); }
            const u32 srow = 2047 - (sel(lane < 32, hi, lo) & 2047);
            const u32x4 xr = __builtin_amdgcn_raw_buffer_load_b128(rq, srow * 2048 + xro, 0, 0);
            u32x2 sg = __builtin_amdgcn_raw_buffer_load_b64(rws, srow * 8, sgb, SC1);
            PIN();
            LDS(u32x2, L_CTL + C_SEL0 * 4) = (u32x2){hi, lo};
            lds_signal(C_ROUTER);
            PIN();
            LDS(u32x4, L_XS + lane * 16) = xr;
            if (!__all(sg.y == tag)) {
                SPIN {
                    asm volatile("" ::: "memory");
                    sg = __builtin_amdgcn_raw_buffer_load_b64(rws, srow * 8, sgb, SC1);
                    if (__all(sg.y == tag)) break;
                }
            }
            LDS(f32x2, L_TOP + 64 + (lane >> 5) * 8) = (f32x2){-lo2f(sg.x), __builtin_amdgcn_rcpf(hi2f(sg.x))};
            lds_signal(C_XSEL);
            __builtin_amdgcn_s_setprio(1);
        } else if (ord == 0) {
            __builtin_amdgcn_s_setprio(3);
            lds_wait(C_ARR, 8);
            __builtin_amdgcn_s_sleep(PDELAY);
            flag_poll(ws, WS_FL + (G * 16 + i) * 128, tag, C_RDY);
            __builtin_amdgcn_s_setprio(1);
        }
        lds_wait(C_ROUTER, 1);
        const u32x2 selk = LDS(u32x2, L_CTL + C_SEL0 * 4);
        const int sel0 = 2047 - (__builtin_amdgcn_readfirstlane(selk.x) & 2047), sel1 = 2047 - (__builtin_amdgcn_readfirstlane(selk.y) & 2047);
        const int lx = lane >> 3, lr = lane & 7;
        const int rowb = I * 128 + sub * 32 + lr, xo = rowb * 2048 + G * 128 + lx * 16;
        f32x2 xn[4][4], sx[4][4];
        {
            lds_wait(C_WALL, 4);
            u32 xst[4];
            u32x4 xq[4];
            SPIN {
                asm volatile("" ::: "memory");
                u32 bad = 0;
#pragma unroll
                for (int z = 0; z < 4; ++z) {
                    const u32x2 t = __builtin_amdgcn_raw_buffer_load_b64(rws, sgb + rowb * 8 + z * 64, 0, SC1);
                    xst[z] = t.x; bad |= t.y ^ tag;
                    xq[z] = __builtin_amdgcn_raw_buffer_load_b128(rq, xo, z * 8 * 2048, 0);
                }
#pragma unroll
                for (int z = 0; z < 4; ++z) asm volatile("" : "+v"(xq[z]));
                if (__all(bad == 0)) break;
            }
            PIN();
#pragma unroll
            for (int z = 0; z < 4; ++z) {
                const float m = lo2f(xst[z]), rs = __builtin_amdgcn_rcpf(hi2f(xst[z]));
#pragma unroll
                for (int e = 0; e < 4; ++e) {
                    const f32x2 x2 = unpk(xq[z][e]);
                    xn[z][e] = rbf2(rbf2(x2 - m) * rs);
                    sx[z][e] = xn[z][e] + x2;
                }
#pragma unroll
                for (int e = 0; e < 4; e += 2) asm volatile("" : "+v"(xn[z][e]), "+v"(xn[z][e + 1]), "+v"(sx[z][e]), "+v"(sx[z][e + 1]));
            }
        }
        u32 oo[4];
#pragma unroll
        for (int z = 0; z < 4; ++z) { const int row = rowb + z * 8; oo[z] = sel(row == sel0 || row == sel1, OOB, xo + z * 8 * 2048); asm volatile("" : "+v"(oo[z])); }
        u32x4 old[2][2];
#pragma unroll
        for (int m = 0; m < 4; ++m) { old[m >> 1][m & 1] = LDS(u32x4, L_OLD + (lx * 2 + (m & 1)) * 1040 + sub * 256 + lr * 16 + (m >> 1) * 128); asm volatile("" : "+v"(old[m >> 1][m & 1])); }
        u32 yo = WS_KV + (G * 16 + lx * 2) * 16384 + I * 1024 + sub * 256 + lr * 16;
        asm volatile("" : "+v"(yo));
        PIN();
        lds_wait(C_YS, 4);
        __builtin_amdgcn_s_sleep(YDELAY);
        u32x4 y[2][2];
#pragma unroll
        for (int m = 0; m < 4; ++m) y[m >> 1][m & 1] = __builtin_amdgcn_raw_buffer_load_b128(rws, yo + (m >> 1) * 128, (m & 1) * 16384, SC1);
        PIN();
        const rsrc_t ro = mkrsrc(out, S * D * 2);
#pragma unroll
        for (int hh = 0; hh < 2; ++hh) {
#pragma unroll
            for (int m = 0; m < 2; ++m) y[hh][m] = ~(old[hh][m] ^ y[hh][m]);
            if (!__all(umax(max4(y[hh][0]), max4(y[hh][1])) != EMPTY)) {
                SPIN {
                    asm volatile("" ::: "memory");
#pragma unroll
                    for (int m = 0; m < 2; ++m) y[hh][m] = ~(old[hh][m] ^ __builtin_amdgcn_raw_buffer_load_b128(rws, yo + hh * 128, m * 16384, SC1));
                    if (__all(umax(max4(y[hh][0]), max4(y[hh][1])) != EMPTY)) break;
                }
            }
#pragma unroll
            for (int zz = 0; zz < 2; ++zz) {
                const int z = zz * 2 + hh;
                u32x4 o;
#pragma unroll
                for (int e = 0; e < 4; ++e) o[e] = pack2v(__builtin_elementwise_fma(xn[z][e], unpk(y[hh][e >> 1][zz * 2 + (e & 1)]), sx[z][e]));
                __builtin_amdgcn_raw_buffer_store_b128(o, ro, oo[z], 0, SC1);
            }
            PIN();
        }
        __builtin_amdgcn_raw_buffer_store_b32(tag, rws, sel(b == 0 && wave == 8 && lane == 0, WS_EPOCH, OOB), 0, SC1);
        __builtin_amdgcn_s_waitcnt(0);
    } else {
        const rsrc_t rws = mkrsrc(ws, WS_BYTES);
#pragma unroll
        for (int r = 0; r < 8; ++r) a.v[r] = __builtin_amdgcn_raw_buffer_load_b32(mkrsrc(v, S * D * 2), vo, r * 2048, NT);
        PIN();
        lds_signal(C_ST0);
        __builtin_amdgcn_s_setprio(2);
        {
            u32x4 o0, o1;
            kv_compute(a, sh, o0, o1);
            PIN();
            const int cc = 2 * i + (g & 1);
            const u32 koff = WS_KV + (G * 16 + (cc >> 2)) * 16384 + (I * 16 + wave * 2 + (lane >> 5)) * 64 + (cc & 3) * 16;
            __builtin_amdgcn_raw_buffer_store_b128(o0, rws, koff, 0, SC1);
            __builtin_amdgcn_raw_buffer_store_b128(o1, rws, koff + 8 * 16384, 0, SC1);
            PIN();
            const int lo = L_OLD + (cc >> 2) * 1040 + (wave * 2 + (lane >> 5)) * 64 + (cc & 3) * 16;
            LDS(u32x4, lo) = o0;
            LDS(u32x4, lo + 8 * 1040) = o1;
            PIN();
        }
        if (opaque_s(wave) >= 4) {
            const int n = i;
            const int fg = wf >> 2, ks = wf & 3;
            const u32 gstr = gi < 28 ? F * 2 : 0;
            const char* gp = (const char*)((n & 8) ? wu : wg) + (ks * 256 + sub * 64 + g * 8) * gstr + sel(gi < 28, fg * 128 + (n & 7) * 16, 0);
            u32x4 GW[2][8], Wd[8];
            u32 wdo = (fg * 64 + (n >> 3) * 32 + g * 8) * 2048 + ks * 512 + sub * 128 + (n & 7) * 16;
            u32 xao = L_XS + umin(n & 3, 2) * 512 + sub * 128 + g * 16, hao = WS_A + wf * 1024 + lane * 16;
            u32 pso = sel(n < 8, WS_P + ((ks * 16 + sub * 4 + (n >> 1)) * 56 + fg) * 128 + (n & 1) * 64 + g * 16, OOB);
            asm volatile("" : "+v"(wdo), "+v"(xao), "+v"(hao), "+v"(pso));
#define GLOAD(w0, w1) _Pragma("unroll") for (int w = w0; w < w1; ++w) { if (w >= GA + WIN) VMWAIT(WIN - 1); \
                GW[w >> 3][w & 7] = __builtin_nontemporal_load((const u32x4*)(gp + ((w >> 3) * 32 + (w & 7)) * gstr)); } PIN();
            lds_signal(C_KST);
            const u32 tag = get_tag();
            VMWAIT(0);
            kv_post(rws, cg, lane, tag);
            lds_wait(C_ARR, 8);
            lds_wait(C_PUB, 1);
            GLOAD(0, GA)
            lds_wait(C_RDY, 1);
            {
                u32x4 uu[4];
#pragma unroll
                for (int m = 0; m < 4; ++m) uu[m] = __builtin_amdgcn_raw_buffer_load_b128(rws, WS_KV + cg * 16384 + sub * 4096 + m * 1024 + lane * 16, 0, SC1);
                PIN();
                lds_signal(C_ISSUED);
                lds_wait(C_ISSUED, 4);
                GLOAD(GA, GB)
#pragma unroll
                for (int m = 0; m < 4; ++m) LDS(u32x4, (lane & 3) * CH_BYTES + (32 + sub * 64 + m * 16 + (lane >> 2)) * 16) = uu[m];
                lds_signal(C_KV);
            }
            __builtin_amdgcn_s_setprio(3);
            if (gi < 28) {
                PIN();
                GLOAD(GB, T0AT)
                tr8(GW[0]);
                GLOAD(T0AT, 16)
#pragma unroll
                for (int w = 0; w < 8; ++w) {
                    if (w < WDWIN) VMWAIT(WIN - 1);
                    Wd[w] = __builtin_amdgcn_raw_buffer_load_b128(mkrsrc(wd, D * F * 2), wdo, w * 2048, NT);
                }
                PIN();
                lds_signal(C_WALL);
                tr8(GW[1]);
                lds_wait(C_XSEL, 1);
                f32x4 acc[8];
#pragma unroll
                for (int c = 0; c < 8; ++c) acc[c] = (f32x4){0.f, 0.f, 0.f, 0.f};
                const f32x4 ns = LDS(f32x4, L_TOP + 64);
#pragma unroll
                for (int s = 0; s < 2; ++s) mm8(acc, LDS(u32x4, xao + s * 64), GW[s]);
                {
                    f32x4 r = pick(acc, g, 0);
                    const f32x4 cs = pick(acc, g, 2);
#pragma unroll
                    for (int e = 0; e < 4; ++e) r[e] = fmaf(ns[(e & 1) * 2], cs[e & 2], r[e]) * ns[(e & 1) * 2 + 1];
                    LDS(f32x4, L_RED + sub * 1024 + lane * 16) = r;
                }
                if (lds_arrive(C_F) == 3) {
                    const f32x4 t = (LDS(f32x4, L_RED + lane * 16) + LDS(f32x4, L_RED + 1024 + lane * 16)) + (LDS(f32x4, L_RED + 2048 + lane * 16) + LDS(f32x4, L_RED + 3072 + lane * 16));
                    __builtin_amdgcn_raw_buffer_store_b128(noempty(t), rws, hao, 0, SC1);
                    PIN();
                    tr8(Wd);
                    __builtin_amdgcn_s_sleep(HDELAY);
                    u32x4 pa[4];
                    SPIN {
                        asm volatile("" ::: "memory");
#pragma unroll
                        for (int m = 0; m < 4; ++m) pa[m] = __builtin_amdgcn_raw_buffer_load_b128(rws, (hao & ~3072u) + m * 1024, 0, SC1);
                        if (__all(umax(umax(max4(pa[0]), max4(pa[1])), umax(max4(pa[2]), max4(pa[3]))) != EMPTY)) break;
                        __builtin_amdgcn_s_sleep(2);
                    }
                    float hv[4];
#pragma unroll
                    for (int e = 0; e < 4; ++e) {
                        const float gg = rbf((__uint_as_float(pa[0][e]) + __uint_as_float(pa[1][e])) + (__uint_as_float(pa[2][e]) + __uint_as_float(pa[3][e])));
                        hv[e] = rbf(gg * __builtin_amdgcn_rcpf(1.0f + __expf(-gg))) * dppf<0x128>(gg);
                    }
                    const int ho = sel(n < 8, L_HB + n * 16 + g * 4, L_RED + lane * 4);
                    LDS(u32, ho) = pack2(hv[0], hv[2]);
                    LDS(u32, ho + 128) = pack2(hv[1], hv[3]);
                    lds_signal(C_HB);
                } else {
                    tr8(Wd);
                    lds_wait(C_HB, 1);
                }
                PIN();
#pragma unroll
                for (int c = 0; c < 8; ++c) acc[c] = (f32x4){0.f, 0.f, 0.f, 0.f};
                mm8(acc, LDS(u32x4, L_HB + (n & 1) * 128 + ((n >> 1) & 1) * 64 + g * 16), Wd);
                f32x4 r = pick(acc, g, 0);
                const f32x4 r2 = pick(acc, g, 2);
#pragma unroll
                for (int e = 0; e < 4; ++e) r[e] += dppf<0x128>(r2[e]);
                __builtin_amdgcn_raw_buffer_store_b128(noempty(r), rws, pso, 0, SC1);
                PIN();
            } else {
                lds_signal(C_WALL);
                lds_wait(C_XSEL, 1);
                lds_wait(C_Y, 4);
                __builtin_amdgcn_s_sleep(40);
            }
            if (sub == 0) {
                const int e = (lane >> 1) & 3, c = 2 * (lane & 1) + (e >> 1);
                const u32 kq = LDS(u32, L_CTL + (C_SEL0 + (e & 1)) * 4);
                const int row = 2047 - (kq & 2047);
                const float x = lo2f(__builtin_amdgcn_raw_buffer_load_b16(mkrsrc(q, S * D * 2), (row * D + cg * 4 + c) * 2, 0, 0));
                const float rv = __uint_as_float((kq ^ (((int)~kq >> 31) | 0x80000000u)) & 0xffff0000u);
                float sig = rbf(__builtin_amdgcn_rcpf(1.0f + __expf(-rv)));
                const f32x2 st = LDS(f32x2, L_TOP + 64 + (e & 1) * 8);
                float xn = rbf(rbf(x + st.x) * st.y);
                lds_wait(C_Y, 4);
                const int gg = (row >> 2) & 3;
                const float y2 = lo2f(LDS(u16, ybase(c) + (row >> 8) * 512 + gg * 128 + ((((row >> 4) & 15) * 8) ^ ((gg >> 1) << 5)) + (row & 3) * 2));
                float base = rbf(x + rbf(xn * y2));
                u32 po = WS_P + ((cg >> 2) * 56 + (lane >> 1)) * 128 + (cg & 3) * 32 + (lane & 1) * 16, po2 = sel(lane < 48, po + 4096, OOB);
                u32 so = sel(lane < 8, (row * D + cg * 4 + c) * 2, OOB);
                asm volatile("" : "+v"(base), "+v"(xn), "+v"(sig), "+v"(po), "+v"(po2), "+v"(so));
                PIN();
                __builtin_amdgcn_s_sleep(HDELAY2);
                u32x4 pp[2];
                SPIN {
                    asm volatile("" ::: "memory");
                    pp[0] = __builtin_amdgcn_raw_buffer_load_b128(rws, po, 0, SC1);
                    pp[1] = __builtin_amdgcn_raw_buffer_load_b128(rws, po2, 0, SC1);
                    if (__all(umax(max4(pp[0]), max4(pp[1])) != EMPTY)) break;
                    __builtin_amdgcn_s_sleep(2);
                }
                float sv[4];
#pragma unroll
                for (int z = 0; z < 4; ++z) sv[z] = __uint_as_float(pp[0][z]) + __uint_as_float(pp[1][z]);
                const float s0 = self(lane & 2, sv[1], sv[0]) + dppf<0x4E>(self(lane & 2, sv[0], sv[1])), s1 = self(lane & 2, sv[3], sv[2]) + dppf<0x4E>(self(lane & 2, sv[2], sv[3]));
                float s = self(lane & 4, s1, s0) + dppf<0x124>(self(lane & 4, s0, s1));
                s += dppf<0x128>(s);
                s = xrow_sum(s);
                const float res = base + rbf(xn + rbf(rbf(s) * sig));
                __builtin_amdgcn_raw_buffer_store_b16((u16)pack2(res, 0.f), mkrsrc(out, S * D * 2), so, 0, 0);
            }
            __builtin_amdgcn_s_waitcnt(0);
        } else {
            const int ub = sub * CH_BYTES;
            LDS(u32x4, ub + sel(lane < 32, lane * 16, U_BYTES + ((lane >> 1) & 3) * HC_BYTES + (lane & 1) * 16)) = (u32x4){0u, 0u, 0u, 0u};
            u32 dw;
            asm volatile("s_load_dword %0, %1, 0x0\n\ts_waitcnt lgkmcnt(0)" : "=s"(dw) : "s"(ssD + cg * 4 + (sub & 2)) : "memory");
            const u32 tag = get_tag();
            lds_wait(C_KST, 4);
            u32x4 hc[4];
#pragma unroll
            for (int m = 0; m < 4; ++m) hc[m] = __builtin_amdgcn_raw_buffer_load_b128(mkrsrc(ssk + (size_t)(cg * 4 + sub) * S, S * 2), lane * 16 + m * 1024, 0, NT);
            VMWAIT(4);
            kv_post(rws, cg, lane, tag);
            h_stage(hc, ub + U_BYTES + (lane + 2) * 16);
            PIN();
            const u32 dbits = (sub & 1) ? dw >> 16 : dw & 0xffffu;
            u32x4 AD;
            { const int jj = 15 - i - 8 * g;
#pragma unroll
              for (int e = 0; e < 4; ++e) AD[e] = sel(jj == 2 * e, dbits, sel(jj == 2 * e + 1, dbits << 16, 0u)); }
            asm volatile("" : "+v"(AD));
            PIN();
            u32 cnt, st;
            asm volatile(
                "v_mov_b32 v144, %[pa0]\n\t"
                "v_mov_b32 v145, %[pb0]\n\t"
                "ds_read_b64 v[40:41], v144 offset:0\n\t"
                "ds_read_b64 v[42:43], v144 offset:8\n\t"
                "ds_read_b64 v[44:45], v144 offset:512\n\t"
                "ds_read_b64 v[46:47], v144 offset:520\n\t"
                "ds_read_b64 v[48:49], v144 offset:1024\n\t"
                "ds_read_b64 v[50:51], v144 offset:1032\n\t"
                "ds_read_b64 v[52:53], v144 offset:1536\n\t"
                "ds_read_b64 v[54:55], v144 offset:1544\n\t"
                "ds_read_b64 v[56:57], v144 offset:2048\n\t"
                "ds_read_b64 v[58:59], v144 offset:2056\n\t"
                "ds_read_b64 v[60:61], v144 offset:2560\n\t"
                "ds_read_b64 v[62:63], v144 offset:2568\n\t"
                "s_waitcnt lgkmcnt(6)\n\t"
                "ds_read_b64 v[64:65], v144 offset:3072\n\t"
                "ds_read_b64 v[66:67], v144 offset:3080\n\t"
                "ds_read_b64 v[68:69], v144 offset:3584\n\t"
                "ds_read_b64 v[70:71], v144 offset:3592\n\t"
                "s_mov_b32 %[cnt], 0x1000000\n\t"
                "4:\n\t"
                "ds_read_b32 v146, %[ctl] offset:64\n\t"
                "s_waitcnt lgkmcnt(0)\n\t"
                "v_readfirstlane_b32 %[st], v146\n\t"
                "s_cmp_ge_u32 %[st], 4\n\t"
                "s_cbranch_scc1 40f\n\t"
                "s_sub_u32 %[cnt], %[cnt], 1\n\t"
                "s_cbranch_scc0 4b\n\t"
                "40:\n\t"
                "s_setprio 0\n\t"
                "ds_read_b128 v[100:103], v145 offset:3584\n\t"
                "ds_read_b128 v[96:99], v145 offset:3072\n\t"
                "ds_read_b128 v[92:95], v145 offset:2560\n\t"
                "ds_read_b128 v[88:91], v145 offset:2048\n\t"
                "ds_read_b128 v[84:87], v145 offset:1536\n\t"
                "ds_read_b128 v[80:83], v145 offset:1024\n\t"
                "ds_read_b128 v[76:79], v145 offset:512\n\t"
                "ds_read_b128 v[72:75], v145 offset:0\n\t"
                "s_waitcnt lgkmcnt(7)\n\t"
                "v_mfma_f32_16x16x32_bf16 v[132:135], %[ad], v[100:103], 0\n\t"
                "s_waitcnt lgkmcnt(6)\n\t"
                "v_mfma_f32_16x16x32_bf16 v[128:131], %[ad], v[96:99], 0\n\t"
                "s_waitcnt lgkmcnt(5)\n\t"
                "v_mfma_f32_16x16x32_bf16 v[124:127], %[ad], v[92:95], 0\n\t"
                "s_waitcnt lgkmcnt(4)\n\t"
                "v_mfma_f32_16x16x32_bf16 v[120:123], %[ad], v[88:91], 0\n\t"
                "s_waitcnt lgkmcnt(3)\n\t"
                "v_mfma_f32_16x16x32_bf16 v[116:119], %[ad], v[84:87], 0\n\t"
                "s_waitcnt lgkmcnt(2)\n\t"
                "v_mfma_f32_16x16x32_bf16 v[112:115], %[ad], v[80:83], 0\n\t"
                "s_waitcnt lgkmcnt(1)\n\t"
                "v_mfma_f32_16x16x32_bf16 v[108:111], %[ad], v[76:79], 0\n\t"
                "s_waitcnt lgkmcnt(0)\n\t"
                "v_mfma_f32_16x16x32_bf16 v[104:107], %[ad], v[72:75], 0\n\t"
                "s_mov_b32 %[cnt], 7\n\t"
                "1:\n\t"
                "s_waitcnt lgkmcnt(9)\n\t"
                "v_mfma_f32_16x16x32_bf16 v[104:107], v[40:43], v[72:75], v[104:107]\n\t"
                "ds_read_b128 v[88:91], v145 offset:2048\n\t"
                "s_waitcnt lgkmcnt(7)\n\t"
                "v_mfma_f32_16x16x32_bf16 v[108:111], v[40:43], v[76:79], v[108:111]\n\t"
                "ds_read_b64 v[56:57], v144 offset:2048\n\t"
                "v_mfma_f32_16x16x32_bf16 v[108:111], v[44:47], v[72:75], v[108:111]\n\t"
                "ds_read_b64 v[58:59], v144 offset:2056\n\t"
                "s_waitcnt lgkmcnt(7)\n\t"
                "v_mfma_f32_16x16x32_bf16 v[112:115], v[48:51], v[72:75], v[112:115]\n\t"
                "ds_read_b128 v[92:95], v145 offset:2560\n\t"
                "v_mfma_f32_16x16x32_bf16 v[112:115], v[44:47], v[76:79], v[112:115]\n\t"
                "ds_read_b64 v[60:61], v144 offset:2560\n\t"
                "s_waitcnt lgkmcnt(8)\n\t"
                "v_mfma_f32_16x16x32_bf16 v[112:115], v[40:43], v[80:83], v[112:115]\n\t"
                "ds_read_b64 v[62:63], v144 offset:2568\n\t"
                "s_waitcnt lgkmcnt(6)\n\t"
                "v_mfma_f32_16x16x32_bf16 v[116:119], v[40:43], v[84:87], v[116:119]\n\t"
                "ds_read_b128 v[96:99], v145 offset:3072\n\t"
                "v_mfma_f32_16x16x32_bf16 v[116:119], v[44:47], v[80:83], v[116:119]\n\t"
                "ds_read_b64 v[64:65], v144 offset:3072\n\t"
                "v_mfma_f32_16x16x32_bf16 v[116:119], v[48:51], v[76:79], v[116:119]\n\t"
                "ds_read_b64 v[66:67], v144 offset:3080\n\t"
                "v_mfma_f32_16x16x32_bf16 v[116:119], v[52:55], v[72:75], v[116:119]\n\t"
                "ds_read_b128 v[100:103], v145 offset:3584\n\t"
                "s_waitcnt lgkmcnt(7)\n\t"
                "v_mfma_f32_16x16x32_bf16 v[120:123], v[56:59], v[72:75], v[120:123]\n\t"
                "ds_read_b64 v[68:69], v144 offset:3584\n\t"
                "v_mfma_f32_16x16x32_bf16 v[120:123], v[52:55], v[76:79], v[120:123]\n\t"
                "ds_read_b64 v[70:71], v144 offset:3592\n\t"
                "v_mfma_f32_16x16x32_bf16 v[120:123], v[48:51], v[80:83], v[120:123]\n\t"
                "v_mfma_f32_16x16x32_bf16 v[120:123], v[44:47], v[84:87], v[120:123]\n\t"
                "v_add_u32 v144, 64, v144\n\t"
                "v_subrev_u32 v145, 64, v145\n\t"
                "v_mfma_f32_16x16x32_bf16 v[120:123], v[40:43], v[88:91], v[120:123]\n\t"
                "s_waitcnt lgkmcnt(4)\n\t"
                "v_mfma_f32_16x16x32_bf16 v[124:127], v[40:43], v[92:95], v[124:127]\n\t"
                "v_mfma_f32_16x16x32_bf16 v[124:127], v[44:47], v[88:91], v[124:127]\n\t"
                "v_mfma_f32_16x16x32_bf16 v[124:127], v[48:51], v[84:87], v[124:127]\n\t"
                "v_mfma_f32_16x16x32_bf16 v[124:127], v[52:55], v[80:83], v[124:127]\n\t"
                "v_mfma_f32_16x16x32_bf16 v[124:127], v[56:59], v[76:79], v[124:127]\n\t"
                "v_mfma_f32_16x16x32_bf16 v[124:127], v[60:63], v[72:75], v[124:127]\n\t"
                "s_waitcnt lgkmcnt(0)\n\t"
                "v_mfma_f32_16x16x32_bf16 v[128:131], v[64:67], v[72:75], v[128:131]\n\t"
                "v_mfma_f32_16x16x32_bf16 v[128:131], v[60:63], v[76:79], v[128:131]\n\t"
                "v_mfma_f32_16x16x32_bf16 v[128:131], v[56:59], v[80:83], v[128:131]\n\t"
                "v_mfma_f32_16x16x32_bf16 v[128:131], v[52:55], v[84:87], v[128:131]\n\t"
                "v_mfma_f32_16x16x32_bf16 v[128:131], v[48:51], v[88:91], v[128:131]\n\t"
                "v_mfma_f32_16x16x32_bf16 v[128:131], v[44:47], v[92:95], v[128:131]\n\t"
                "v_mfma_f32_16x16x32_bf16 v[128:131], v[40:43], v[96:99], v[128:131]\n\t"
                "v_mfma_f32_16x16x32_bf16 v[132:135], v[40:43], v[100:103], v[132:135]\n\t"
                "ds_read_b64 v[40:41], v144 offset:0\n\t"
                "ds_read_b64 v[42:43], v144 offset:8\n\t"
                "v_mfma_f32_16x16x32_bf16 v[132:135], v[68:71], v[72:75], v[132:135]\n\t"
                "ds_read_b128 v[72:75], v145 offset:0\n\t"
                "v_mfma_f32_16x16x32_bf16 v[132:135], v[44:47], v[96:99], v[132:135]\n\t"
                "ds_read_b64 v[44:45], v144 offset:512\n\t"
                "ds_read_b64 v[46:47], v144 offset:520\n\t"
                "v_mfma_f32_16x16x32_bf16 v[132:135], v[64:67], v[76:79], v[132:135]\n\t"
                "ds_read_b128 v[76:79], v145 offset:512\n\t"
                "v_mfma_f32_16x16x32_bf16 v[132:135], v[48:51], v[92:95], v[132:135]\n\t"
                "ds_read_b64 v[48:49], v144 offset:1024\n\t"
                "ds_read_b64 v[50:51], v144 offset:1032\n\t"
                "v_mfma_f32_16x16x32_bf16 v[132:135], v[60:63], v[80:83], v[132:135]\n\t"
                "ds_read_b128 v[80:83], v145 offset:1024\n\t"
                "v_mfma_f32_16x16x32_bf16 v[132:135], v[52:55], v[88:91], v[132:135]\n\t"
                "ds_read_b64 v[52:53], v144 offset:1536\n\t"
                "ds_read_b64 v[54:55], v144 offset:1544\n\t"
                "v_mfma_f32_16x16x32_bf16 v[132:135], v[56:59], v[84:87], v[132:135]\n\t"
                "ds_read_b128 v[84:87], v145 offset:1536\n\t"
                "s_sub_u32 %[cnt], %[cnt], 1\n\t"
                "s_cmp_lg_u32 %[cnt], 0\n\t"
                "s_cbranch_scc1 1b\n\t"
                "s_waitcnt lgkmcnt(9)\n\t"
                "v_mfma_f32_16x16x32_bf16 v[104:107], v[40:43], v[72:75], v[104:107]\n\t"
                "ds_read_b128 v[88:91], v145 offset:2048\n\t"
                "s_waitcnt lgkmcnt(7)\n\t"
                "v_mfma_f32_16x16x32_bf16 v[108:111], v[40:43], v[76:79], v[108:111]\n\t"
                "ds_read_b64 v[56:57], v144 offset:2048\n\t"
                "v_mfma_f32_16x16x32_bf16 v[108:111], v[44:47], v[72:75], v[108:111]\n\t"
                "ds_read_b64 v[58:59], v144 offset:2056\n\t"
                "s_waitcnt lgkmcnt(7)\n\t"
                "v_mfma_f32_16x16x32_bf16 v[112:115], v[48:51], v[72:75], v[112:115]\n\t"
                "ds_read_b128 v[92:95], v145 offset:2560\n\t"
                "v_mfma_f32_16x16x32_bf16 v[112:115], v[44:47], v[76:79], v[112:115]\n\t"
                "ds_read_b64 v[60:61], v144 offset:2560\n\t"
                "s_waitcnt lgkmcnt(8)\n\t"
                "v_mfma_f32_16x16x32_bf16 v[112:115], v[40:43], v[80:83], v[112:115]\n\t"
                "ds_read_b64 v[62:63], v144 offset:2568\n\t"
                "s_waitcnt lgkmcnt(6)\n\t"
                "v_mfma_f32_16x16x32_bf16 v[116:119], v[40:43], v[84:87], v[116:119]\n\t"
                "ds_read_b128 v[96:99], v145 offset:3072\n\t"
                "v_mfma_f32_16x16x32_bf16 v[116:119], v[44:47], v[80:83], v[116:119]\n\t"
                "ds_read_b64 v[64:65], v144 offset:3072\n\t"
                "v_mfma_f32_16x16x32_bf16 v[116:119], v[48:51], v[76:79], v[116:119]\n\t"
                "ds_read_b64 v[66:67], v144 offset:3080\n\t"
                "v_mfma_f32_16x16x32_bf16 v[116:119], v[52:55], v[72:75], v[116:119]\n\t"
                "ds_read_b128 v[100:103], v145 offset:3584\n\t"
                "s_waitcnt lgkmcnt(7)\n\t"
                "v_mfma_f32_16x16x32_bf16 v[120:123], v[56:59], v[72:75], v[120:123]\n\t"
                "ds_read_b64 v[68:69], v144 offset:3584\n\t"
                "v_mfma_f32_16x16x32_bf16 v[120:123], v[52:55], v[76:79], v[120:123]\n\t"
                "ds_read_b64 v[70:71], v144 offset:3592\n\t"
                "v_mfma_f32_16x16x32_bf16 v[120:123], v[48:51], v[80:83], v[120:123]\n\t"
                "v_mfma_f32_16x16x32_bf16 v[120:123], v[44:47], v[84:87], v[120:123]\n\t"
                "v_mfma_f32_16x16x32_bf16 v[120:123], v[40:43], v[88:91], v[120:123]\n\t"
                "v_cvt_pk_bf16_f32 v136, v104, v105\n\t"
                "v_cvt_pk_bf16_f32 v137, v106, v107\n\t"
                "ds_write_b64 %[y0], v[136:137] offset:0\n\t"
                "s_waitcnt lgkmcnt(5)\n\t"
                "v_mfma_f32_16x16x32_bf16 v[124:127], v[40:43], v[92:95], v[124:127]\n\t"
                "v_mfma_f32_16x16x32_bf16 v[124:127], v[44:47], v[88:91], v[124:127]\n\t"
                "v_cvt_pk_bf16_f32 v138, v108, v109\n\t"
                "v_cvt_pk_bf16_f32 v139, v110, v111\n\t"
                "ds_write_b64 %[y0], v[138:139] offset:512\n\t"
                "v_mfma_f32_16x16x32_bf16 v[124:127], v[48:51], v[84:87], v[124:127]\n\t"
                "v_mfma_f32_16x16x32_bf16 v[124:127], v[52:55], v[80:83], v[124:127]\n\t"
                "v_cvt_pk_bf16_f32 v140, v112, v113\n\t"
                "v_cvt_pk_bf16_f32 v141, v114, v115\n\t"
                "ds_write_b64 %[y0], v[140:141] offset:1024\n\t"
                "v_mfma_f32_16x16x32_bf16 v[124:127], v[56:59], v[76:79], v[124:127]\n\t"
                "v_mfma_f32_16x16x32_bf16 v[124:127], v[60:63], v[72:75], v[124:127]\n\t"
                "v_cvt_pk_bf16_f32 v142, v116, v117\n\t"
                "v_cvt_pk_bf16_f32 v143, v118, v119\n\t"
                "ds_write_b64 %[y0], v[142:143] offset:1536\n\t"
                "s_waitcnt lgkmcnt(4)\n\t"
                "v_mfma_f32_16x16x32_bf16 v[128:131], v[64:67], v[72:75], v[128:131]\n\t"
                "v_mfma_f32_16x16x32_bf16 v[128:131], v[60:63], v[76:79], v[128:131]\n\t"
                "v_mfma_f32_16x16x32_bf16 v[128:131], v[56:59], v[80:83], v[128:131]\n\t"
                "v_mfma_f32_16x16x32_bf16 v[128:131], v[52:55], v[84:87], v[128:131]\n\t"
                "v_cvt_pk_bf16_f32 v136, v120, v121\n\t"
                "v_cvt_pk_bf16_f32 v137, v122, v123\n\t"
                "ds_write_b64 %[y0], v[136:137] offset:2048\n\t"
                "v_mfma_f32_16x16x32_bf16 v[128:131], v[48:51], v[88:91], v[128:131]\n\t"
                "v_mfma_f32_16x16x32_bf16 v[128:131], v[44:47], v[92:95], v[128:131]\n\t"
                "v_mfma_f32_16x16x32_bf16 v[128:131], v[40:43], v[96:99], v[128:131]\n\t"
                "v_mfma_f32_16x16x32_bf16 v[132:135], v[40:43], v[100:103], v[132:135]\n\t"
                "v_mfma_f32_16x16x32_bf16 v[132:135], v[68:71], v[72:75], v[132:135]\n\t"
                "v_mfma_f32_16x16x32_bf16 v[132:135], v[44:47], v[96:99], v[132:135]\n\t"
                "v_cvt_pk_bf16_f32 v138, v124, v125\n\t"
                "v_cvt_pk_bf16_f32 v139, v126, v127\n\t"
                "ds_write_b64 %[y0], v[138:139] offset:2560\n\t"
                "v_mfma_f32_16x16x32_bf16 v[132:135], v[64:67], v[76:79], v[132:135]\n\t"
                "v_mfma_f32_16x16x32_bf16 v[132:135], v[48:51], v[92:95], v[132:135]\n\t"
                "v_mfma_f32_16x16x32_bf16 v[132:135], v[60:63], v[80:83], v[132:135]\n\t"
                "v_mfma_f32_16x16x32_bf16 v[132:135], v[52:55], v[88:91], v[132:135]\n\t"
                "v_mfma_f32_16x16x32_bf16 v[132:135], v[56:59], v[84:87], v[132:135]\n\t"
                "s_setprio 2\n\t"
                "s_nop 3\n\t"
                "v_cvt_pk_bf16_f32 v40, v128, v129\n\t"
                "v_cvt_pk_bf16_f32 v41, v130, v131\n\t"
                "ds_write_b64 %[y0], v[40:41] offset:3072\n\t"
                "v_cvt_pk_bf16_f32 v42, v132, v133\n\t"
                "v_cvt_pk_bf16_f32 v43, v134, v135\n\t"
                "ds_write_b64 %[y0], v[42:43] offset:3584\n\t"
                "s_mov_b64 exec, 1\n\t"
                "ds_add_u32 %[ctl], %[one] offset:56\n\t"
                "s_mov_b64 exec, -1\n\t"
                "ds_read_b128 v[48:51], %[uo] offset:0\n\t"
                "ds_read_b128 v[52:55], %[uo] offset:256\n\t"
                "ds_read_b128 v[56:59], %[uo] offset:512\n\t"
                "ds_read_b128 v[60:63], %[uo] offset:768\n\t"
                "s_mov_b32 %[cnt], 0x1000000\n\t"
                "5:\n\t"
                "ds_read_b32 v146, %[ctl] offset:56\n\t"
                "s_waitcnt lgkmcnt(0)\n\t"
                "v_readfirstlane_b32 %[st], v146\n\t"
                "s_cmp_ge_u32 %[st], 4\n\t"
                "s_cbranch_scc1 50f\n\t"
                "s_sub_u32 %[cnt], %[cnt], 1\n\t"
                "s_cbranch_scc0 5b\n\t"
                "50:\n\t"
                "ds_read_b64_tr_b16 v[72:73], %[yb] offset:0\n\t"
                "ds_read_b64_tr_b16 v[74:75], %[yb] offset:8\n\t"
                "ds_read_b64_tr_b16 v[76:77], %[yb] offset:64\n\t"
                "ds_read_b64_tr_b16 v[78:79], %[yb] offset:72\n\t"
                "ds_read_b64_tr_b16 v[80:81], %[yb] offset:512\n\t"
                "ds_read_b64_tr_b16 v[82:83], %[yb] offset:520\n\t"
                "ds_read_b64_tr_b16 v[84:85], %[yb] offset:576\n\t"
                "ds_read_b64_tr_b16 v[86:87], %[yb] offset:584\n\t"
                "s_waitcnt lgkmcnt(6)\n\t"
                "v_min_u32 v72, -2, v72\n\t"
                "v_min_u32 v73, -2, v73\n\t"
                "v_min_u32 v74, -2, v74\n\t"
                "v_min_u32 v75, -2, v75\n\t"
                "v_xnor_b32 v72, v48, v72\n\t"
                "v_xnor_b32 v73, v49, v73\n\t"
                "v_xnor_b32 v74, v50, v74\n\t"
                "v_xnor_b32 v75, v51, v75\n\t"
                "buffer_store_dwordx4 v[72:75], %[yd], %[srd], 0 offen offset:0 sc1\n\t"
                "s_waitcnt lgkmcnt(4)\n\t"
                "v_min_u32 v76, -2, v76\n\t"
                "v_min_u32 v77, -2, v77\n\t"
                "v_min_u32 v78, -2, v78\n\t"
                "v_min_u32 v79, -2, v79\n\t"
                "v_xnor_b32 v76, v52, v76\n\t"
                "v_xnor_b32 v77, v53, v77\n\t"
                "v_xnor_b32 v78, v54, v78\n\t"
                "v_xnor_b32 v79, v55, v79\n\t"
                "buffer_store_dwordx4 v[76:79], %[yd], %[srd], 0 offen offset:1024 sc1\n\t"
                "s_waitcnt lgkmcnt(2)\n\t"
                "v_min_u32 v80, -2, v80\n\t"
                "v_min_u32 v81, -2, v81\n\t"
                "v_min_u32 v82, -2, v82\n\t"
                "v_min_u32 v83, -2, v83\n\t"
                "v_xnor_b32 v80, v56, v80\n\t"
                "v_xnor_b32 v81, v57, v81\n\t"
                "v_xnor_b32 v82, v58, v82\n\t"
                "v_xnor_b32 v83, v59, v83\n\t"
                "buffer_store_dwordx4 v[80:83], %[yd], %[srd], 0 offen offset:2048 sc1\n\t"
                "s_waitcnt lgkmcnt(0)\n\t"
                "v_min_u32 v84, -2, v84\n\t"
                "v_min_u32 v85, -2, v85\n\t"
                "v_min_u32 v86, -2, v86\n\t"
                "v_min_u32 v87, -2, v87\n\t"
                "v_xnor_b32 v84, v60, v84\n\t"
                "v_xnor_b32 v85, v61, v85\n\t"
                "v_xnor_b32 v86, v62, v86\n\t"
                "v_xnor_b32 v87, v63, v87\n\t"
                "buffer_store_dwordx4 v[84:87], %[yd], %[srd], 0 offen offset:3072 sc1\n\t"
                "s_waitcnt vmcnt(0)\n\t"
                "s_mov_b64 exec, 1\n\t"
                "ds_add_u32 %[ctl], %[one] offset:116\n\t"
                "s_mov_b64 exec, -1\n\t"
                : [cnt] "=&s"(cnt), [st] "=&s"(st)
                : [ad] "v"(AD), [pa0] "v"(ub + U_BYTES + (i & 3) * HC_BYTES + ((i >> 2) + 2 * g + 1) * 8), [pb0] "v"(ub + (32 + 2 * i + 1 - g) * 16), [ctl] "v"(L_CTL), [one] "v"(1u),
                  [y0] "v"(ybase(sub) + g * 128 + ((i * 8) ^ ((g >> 1) << 5))), [yb] "v"(ybase((lane >> 2) & 3) + sub * 1024 + (lane & 3) * 128 + ((g * 16) ^ ((lane & 2) << 4))),
                  [srd] "s"(srd4(ws)), [yd] "v"(WS_KV + cg * 16384 + sub * 4096 + lane * 16), [uo] "v"((lane & 3) * CH_BYTES + (32 + sub * 64 + (lane >> 2)) * 16)
                : "memory", "scc", "v40", "v41", "v42", "v43", "v44", "v45", "v46", "v47", "v48", "v49", "v50", "v51", "v52", "v53", "v54", "v55", "v56", "v57", "v58", "v59", "v60", "v61", "v62", "v63", "v64", "v65", "v66", "v67", "v68", "v69", "v70", "v71", "v72", "v73", "v74", "v75", "v76", "v77", "v78", "v79", "v80", "v81", "v82", "v83", "v84", "v85", "v86", "v87", "v88", "v89", "v90", "v91", "v92", "v93", "v94", "v95", "v96", "v97", "v98", "v99", "v100", "v101", "v102", "v103", "v104", "v105", "v106", "v107", "v108", "v109", "v110", "v111", "v112", "v113", "v114", "v115", "v116", "v117", "v118", "v119", "v120", "v121", "v122", "v123", "v124", "v125", "v126", "v127", "v128", "v129", "v130", "v131", "v132", "v133", "v134", "v135", "v136", "v137", "v138", "v139", "v140", "v141", "v142", "v143", "v144", "v145", "v146");
            __builtin_amdgcn_s_waitcnt(0);
        }
    }
}

extern "C" void kernel_launch(void* const* d_in, const int* in_sizes, int n_in, void* d_out, int out_size, void* d_ws, size_t ws_size,
                              hipStream_t stream) {
    (void)hipFuncSetAttribute((const void*)fused_kernel, hipFuncAttributeMaxDynamicSharedMemorySize, L_END);
    fused_kernel<<<256, 768, L_END, stream>>>((const u16*)d_in[0], (const u16*)d_in[1], (const u16*)d_in[2], (const u16*)d_in[3], (const u16*)d_in[4],
                                              (const u16*)d_in[5], (const u16*)d_in[7], (const u16*)d_in[8], (const u16*)d_in[9], (const u16*)d_in[10],
                                              (u16*)d_out, (char*)d_ws);
}
```
